```python
import torch

def _softmax(x, axis=-1):
    return torch.softmax(x, dim=axis)
def _gelu(x):
    return torch.nn.functional.gelu(x, approximate="tanh")
def _silu(x):
    return torch.nn.functional.silu(x)
def _sigmoid(x):
    return torch.sigmoid(x)
def _layernorm(x, axis=-1, eps=1e-5):
    m = x.mean(dim=axis, keepdim=True)
    v = x.var(dim=axis, keepdim=True, unbiased=False)
    return (x - m) / torch.sqrt(v + eps)
def _scatter_add(tgt, idx, src, axis=0):
    return tgt.clone().index_add_(axis, idx, src)
def _segment_sum(src, seg, n):
    out = torch.zeros((n,) + src.shape[1:], dtype=src.dtype)
    return out.index_add_(0, seg, src)
def _pad(x, pw):
    flat = tuple(v for pair in reversed(pw) for v in pair)
    return torch.nn.functional.pad(x, flat)

kernel_name = 'concat_heads__gqa_block_cached__gqa_block_cached__near_ridge'

def setup_inputs():

    _w = (lambda a: (lambda t: t.to(torch.bfloat16) if t.is_floating_point() else t)(torch.as_tensor(a)))
    def _A_setup():
        B, Sq, D = 8, 64, 448
        Hq, Hkv, Sk, Dh = 16, 8, 1024, 256
        assert Hq % Hkv == 0, (Hq, Hkv)
        return {
            "x": _w((lambda *s: torch.randn(*s, dtype=torch.bfloat16) * 0.8)(B, Sq, D)),
            "k_cache": _w((lambda *s: torch.randn(*s, dtype=torch.bfloat16) * 0.8)(B, Hkv, Sk, Dh)),
            "v_cache": _w((lambda *s: torch.randn(*s, dtype=torch.bfloat16) * 0.05)(B, Hkv, Sk, Dh)),
            "wq": _w((lambda *s: torch.randn(*s, dtype=torch.bfloat16) * 0.8)(D, Hq * Dh) * (D ** -0.5)),
            "wo": _w((lambda *s: torch.randn(*s, dtype=torch.bfloat16) * 0.05)(Hq * Dh, D) * ((Hq * Dh) ** -0.5)),
        }
    _A_in = _A_setup()

    def _B_setup():
        B, Sq, D = 8, 64, 448
        Hq, Hkv, Sk, Dh = 16, 8, 1024, 256
        assert Hq % Hkv == 0, (Hq, Hkv)
        return {
            "x": _w((lambda *s: torch.randn(*s, dtype=torch.bfloat16) * 0.8)(B, Sq, D)),
            "k_cache": _w((lambda *s: torch.randn(*s, dtype=torch.bfloat16) * 0.8)(B, Hkv, Sk, Dh)),
            "v_cache": _w((lambda *s: torch.randn(*s, dtype=torch.bfloat16) * 0.05)(B, Hkv, Sk, Dh)),
            "wq": _w((lambda *s: torch.randn(*s, dtype=torch.bfloat16) * 0.8)(D, Hq * Dh) * (D ** -0.5)),
            "wo": _w((lambda *s: torch.randn(*s, dtype=torch.bfloat16) * 0.05)(Hq * Dh, D) * ((Hq * Dh) ** -0.5)),
        }
    _B_in = _B_setup()

    return {
        "A_x": _A_in["x"],
        "A_k_cache": _A_in["k_cache"],
        "A_v_cache": _A_in["v_cache"],
        "A_wq": _A_in["wq"],
        "A_wo": _A_in["wo"],
        "B_x": _B_in["x"],
        "B_k_cache": _B_in["k_cache"],
        "B_v_cache": _B_in["v_cache"],
        "B_wq": _B_in["wq"],
        "B_wo": _B_in["wo"],
    }

def reference(A_x, A_k_cache, A_v_cache, A_wq, A_wo, B_x, B_k_cache, B_v_cache, B_wq, B_wo):
    def _A_ref(x, k_cache, v_cache, wq, wo):
        B, Sq, D = x.shape
        Hq, Dh = 16, 256
        Hkv = k_cache.shape[1]
        rep = Hq // Hkv
        q = (lambda x, *d: x.permute(*d))((x @ wq).reshape(B, Sq, Hq, Dh), 0, 2, 1, 3)
        k = (lambda x, n, axis: torch.repeat_interleave(x, n, dim=axis))(k_cache, rep, axis=1)
        v = (lambda x, n, axis: torch.repeat_interleave(x, n, dim=axis))(v_cache, rep, axis=1)
        scores = torch.einsum("bhqd,bhkd->bhqk", q, k) * (Dh ** -0.5)
        p = _softmax(scores, axis=-1)
        o = torch.einsum("bhqk,bhkd->bhqd", p, v)
        return (lambda x, *d: x.permute(*d))(o, 0, 2, 1, 3).reshape(B, Sq, Hq * Dh) @ wo

    def _B_ref(x, k_cache, v_cache, wq, wo):
        B, Sq, D = x.shape
        Hq, Dh = 16, 256
        Hkv = k_cache.shape[1]
        rep = Hq // Hkv
        q = (lambda x, *d: x.permute(*d))((x @ wq).reshape(B, Sq, Hq, Dh), 0, 2, 1, 3)
        k = (lambda x, n, axis: torch.repeat_interleave(x, n, dim=axis))(k_cache, rep, axis=1)
        v = (lambda x, n, axis: torch.repeat_interleave(x, n, dim=axis))(v_cache, rep, axis=1)
        scores = torch.einsum("bhqd,bhkd->bhqk", q, k) * (Dh ** -0.5)
        p = _softmax(scores, axis=-1)
        o = torch.einsum("bhqk,bhkd->bhqd", p, v)
        return (lambda x, *d: x.permute(*d))(o, 0, 2, 1, 3).reshape(B, Sq, Hq * Dh) @ wo

    a = _A_ref(A_x, A_k_cache, A_v_cache, A_wq, A_wo)
    b = _B_ref(B_x, B_k_cache, B_v_cache, B_wq, B_wo)
    return torch.concatenate([a, b], axis=-1)
```

```cpp
#include <hip/hip_runtime.h>
#include <hip/hip_bf16.h>
typedef unsigned short u16;
__device__ __forceinline__ float bf2f(u16 v){ return __uint_as_float(((unsigned)v)<<16); }
__device__ __forceinline__ u16 f2bf(float f){ __hip_bfloat16 b = __float2bfloat16(f); return *reinterpret_cast<u16*>(&b); }

__global__ void qproj(const u16* x, const u16* wq, u16* q){
  int n = blockIdx.x*256 + threadIdx.x; int row = blockIdx.y;
  float acc=0; for(int k=0;k<448;++k) acc += bf2f(x[row*448+k])*bf2f(wq[k*4096+n]);
  q[row*4096+n]=f2bf(acc);
}
__global__ void attn(const u16* q, const u16* kc, const u16* vc, u16* o){
  __shared__ float qs[256]; __shared__ float s[1024]; __shared__ float red[256];
  int row = blockIdx.x, h = blockIdx.y; int b = row/64; int kvh = h/2; int t = threadIdx.x;
  qs[t] = bf2f(q[row*4096+h*256+t]); __syncthreads();
  const u16* K = kc + (size_t)(b*8+kvh)*1024*256; const u16* V = vc + (size_t)(b*8+kvh)*1024*256;
  float mx=-1e30f;
  for(int j=t;j<1024;j+=256){ float a=0; for(int d=0;d<256;++d) a+=qs[d]*bf2f(K[j*256+d]); a*=0.0625f; s[j]=a; mx=fmaxf(mx,a);}
  red[t]=mx; __syncthreads();
  for(int o2=128;o2>0;o2>>=1){ if(t<o2) red[t]=fmaxf(red[t],red[t+o2]); __syncthreads(); }
  mx=red[0]; __syncthreads();
  float sum=0; for(int j=t;j<1024;j+=256){ float e=__expf(s[j]-mx); s[j]=e; sum+=e; }
  red[t]=sum; __syncthreads();
  for(int o2=128;o2>0;o2>>=1){ if(t<o2) red[t]+=red[t+o2]; __syncthreads(); }
  sum=red[0];
  float acc=0; for(int j=0;j<1024;++j) acc+=s[j]*bf2f(V[j*256+t]);
  o[row*4096+h*256+t]=f2bf(acc/sum);
}
__global__ void oproj(const u16* o, const u16* wo, u16* out, int coloff){
  int n = threadIdx.x; int row = blockIdx.x;
  if(n>=448) return;
  float acc=0; for(int k=0;k<4096;++k) acc += bf2f(o[row*4096+k])*bf2f(wo[k*448+n]);
  out[row*896+coloff+n]=f2bf(acc);
}
extern "C" void kernel_launch(void* const* d_in, const int* in_sizes, int n_in,
                              void* d_out, int out_size, void* d_ws, size_t ws_size,
                              hipStream_t stream) {
  u16* ws=(u16*)d_ws;
  for(int br=0;br<2;++br){
    const u16* x=(const u16*)d_in[br*5+0]; const u16* kc=(const u16*)d_in[br*5+1]; const u16* vc=(const u16*)d_in[br*5+2];
    const u16* wq=(const u16*)d_in[br*5+3]; const u16* wo=(const u16*)d_in[br*5+4];
    u16* q=ws+br*2*512*4096; u16* o=q+512*4096;
    qproj<<<dim3(16,512),256,0,stream>>>(x,wq,q);
    attn<<<dim3(512,16),256,0,stream>>>(q,kc,vc,o);
    oproj<<<512,448,0,stream>>>(o,wo,(u16*)d_out,br*448);
  }
}
```

```cpp
#include <hip/hip_runtime.h>
#include <utility>

typedef __bf16   bf16x8 __attribute__((ext_vector_type(8)));
typedef __bf16   bf16x4 __attribute__((ext_vector_type(4)));
typedef __bf16   bf16x2 __attribute__((ext_vector_type(2)));
typedef float    f32x4  __attribute__((ext_vector_type(4)));
typedef float    f32x2  __attribute__((ext_vector_type(2)));
typedef unsigned u32x4  __attribute__((ext_vector_type(4)));
typedef unsigned u32x2  __attribute__((ext_vector_type(2)));
typedef unsigned long long u64;
typedef __amdgpu_buffer_rsrc_t srd_t;
#define LAS __attribute__((address_space(3)))
#define DI __device__ __forceinline__
#define PIN() __builtin_amdgcn_sched_barrier(0)
template <int... Is> using iseq = std::integer_sequence<int, Is...>;
template <int N> using mkseq = std::make_integer_sequence<int, N>;

#define STAMP 0

constexpr unsigned WS_EPOCH = 0;
constexpr unsigned WS_FLAG  = 4096;
constexpr unsigned WS_LSE   = WS_FLAG + 32768;
constexpr unsigned WS_PART  = 1u << 20;
constexpr unsigned WS_PART_BYTES = 256u * 7u * 8192u;
constexpr unsigned WS_END   = WS_PART + WS_PART_BYTES;
constexpr unsigned WS_STAMP = 32u << 20;
constexpr int NSTAMP = 7;

constexpr int PT_H = 1, PT_A = 15, PT_SM = 31, PT_B = 35, PT_C = 51, NPT = 65;
enum { IT_X = 0, IT_W = 1, IT_S = 2, IT_WO = 3, IT_TOUCH = 4, IT_LSE = 5 };
struct Item { int type, idx, pt, ops; };
struct Sched { Item it[96]; int n; };
constexpr int slotS(int n)  { constexpr int c[10] = {6, 7, 8, 9, 4, 5, 2, 3, 0, 1}; return c[n % 10]; }
constexpr int slotWO(int i) { constexpr int c[10] = {8, 9, 4, 5, 6, 7, 0, 1, 2, 3}; return c[i % 10]; }
constexpr int slotW(int t, int hh) { return 2 * (t % 3) + hh; }
constexpr int ptS[32] = {
    PT_H + 1, PT_H + 1, PT_H + 5, PT_H + 5, PT_H + 12, PT_H + 12, PT_A + 0,
    PT_A + 0, PT_A + 1, PT_A + 3, PT_A + 5, PT_A + 7, PT_A + 8, PT_A + 9, PT_A + 10, PT_A + 11,
    PT_A + 12, PT_A + 13, PT_A + 14, PT_A + 15, PT_SM + 1, PT_SM + 2, PT_SM + 3,
    PT_B + 0, PT_B + 1, PT_B + 3, PT_B + 5, PT_B + 7, PT_B + 9, PT_B + 10, PT_B + 11, PT_B + 12};
constexpr int ptWO[28] = {
    PT_B + 12, PT_B + 13, PT_B + 13, PT_B + 14, PT_B + 15, PT_C + 0,
    PT_C + 1, PT_C + 1, PT_C + 1, PT_C + 2, PT_C + 2, PT_C + 2, PT_C + 3, PT_C + 3, PT_C + 3, PT_C + 4, PT_C + 4, PT_C + 4,
    PT_C + 5, PT_C + 5, PT_C + 6, PT_C + 6, PT_C + 7, PT_C + 7, PT_C + 8, PT_C + 8, PT_C + 9, PT_C + 9};
constexpr int PT_TOUCH = PT_H + 8;
constexpr int PT_LSE_LD = PT_B + 8, PT_LSE_USE = PT_B + 12;
constexpr int ptW(int t) { return t < 3 ? 0 : PT_H + t - 2; }
constexpr bool drains(int p) { return p <= PT_A || p >= PT_B + 10; }

constexpr Sched make_sched() {
  Sched s{}; int n = 0;
  for (int p = 0; p < NPT; ++p) {
    if (p == 0) {
      s.it[n++] = Item{IT_X, 0, 0, 7};
      s.it[n++] = Item{IT_W, 0, 0, 4};
      s.it[n++] = Item{IT_W, 1, 0, 4};
      s.it[n++] = Item{IT_W, 2, 0, 4};
      continue;
    }
    for (int t = 3; t < 14; ++t) if (ptW(t) == p) s.it[n++] = Item{IT_W, t, p, 4};
    for (int i = 0; i < 32; ++i) if (ptS[i] == p) s.it[n++] = Item{IT_S, i, p, 2};
    for (int i = 0; i < 28; ++i) if (ptWO[i] == p) s.it[n++] = Item{IT_WO, i, p, 2};
    if (p == PT_TOUCH) s.it[n++] = Item{IT_TOUCH, 0, p, 1};
    if (p == PT_LSE_LD) s.it[n++] = Item{IT_LSE, 0, p, 1};
  }
  s.n = n; return s;
}
constexpr Sched SCH = make_sched();
constexpr int find_item(int type, int idx) { for (int i = 0; i < SCH.n; ++i) if (SCH.it[i].type == type && SCH.it[i].idx == idx) return i; return -1000; }
constexpr int ops_after(int L, int p) { int c = 0; for (int i = L + 1; i < SCH.n; ++i) if (SCH.it[i].pt < p) c += SCH.it[i].ops; return c; }
constexpr int imax(int a, int b) { return a > b ? a : b; }
constexpr int need_item(int p) {
  int L = -1000;
  if (p >= PT_H && p < PT_A) L = find_item(IT_W, p - PT_H);
  else if (p >= PT_A && p < PT_SM) L = find_item(IT_S, p - PT_A);
  else if (p >= PT_B && p < PT_C) L = find_item(IT_S, 16 + p - PT_B);
  else if (p >= PT_C) L = imax(find_item(IT_WO, 2 * (p - PT_C)), find_item(IT_WO, 2 * (p - PT_C) + 1));
  if (p == PT_LSE_USE) L = imax(L, find_item(IT_LSE, 0));
  return L;
}
constexpr int wait_cnt(int p) { const int c = ops_after(need_item(p), p); return c > 63 ? 63 : c; }
constexpr bool is_bar(int p) { return p >= 1 && !(p >= PT_SM && p < PT_B); }
constexpr int free_at(int use) {
  int p = use + 1; while (p < NPT && !is_bar(p)) ++p;
  if (p < NPT && (drains(p) || p != use + 1)) return p;
  ++p; while (p < NPT && !is_bar(p)) ++p;
  return p;
}
constexpr bool sched_ok() {
  for (int p = 1; p < NPT; ++p) { if (!is_bar(p)) continue; int L = need_item(p); if (L < 0 || SCH.it[L].pt >= p) return false; }
  int fr[10] = {0, 0, 0, 0, 0, 0, PT_H + 1, PT_H + 1, PT_H + 1, PT_H + 1};
  for (int i = 0; i < SCH.n; ++i) {
    const Item& I = SCH.it[i]; int sl[2] = {-1, -1}; int use = 0;
    if (I.type == IT_W) { sl[0] = slotW(I.idx, 0); sl[1] = slotW(I.idx, 1); use = PT_H + I.idx; }
    else if (I.type == IT_S) { sl[0] = slotS(I.idx); use = I.idx < 16 ? PT_A + I.idx : PT_B + I.idx - 16;
      if (sl[0] < 2 && I.pt <= PT_A) return false;
      if (sl[0] < 4 && free_at(use) > PT_B + 15) return false; }
    else if (I.type == IT_WO) { sl[0] = slotWO(I.idx); use = PT_C + I.idx / 2; if (sl[0] < 4 && I.pt <= PT_C) return false; }
    for (int k = 0; k < 2; ++k) if (sl[k] >= 0) { if (I.pt < fr[sl[k]]) return false; fr[sl[k]] = free_at(use); }
  }
  if (slotW(12, 0) != 0) return false;
  return true;
}
static_assert(sched_ok(), "schedule");
constexpr int npieces(int p) { int c = 0; for (int i = 0; i < SCH.n; ++i) if (SCH.it[i].pt == p) c += SCH.it[i].ops; return c; }
constexpr int piece_item(int p, int k) { for (int i = 0; i < SCH.n; ++i) if (SCH.it[i].pt == p) { if (k < SCH.it[i].ops) return i; k -= SCH.it[i].ops; } return -1; }
constexpr int piece_sub(int p, int k) { for (int i = 0; i < SCH.n; ++i) if (SCH.it[i].pt == p) { if (k < SCH.it[i].ops) return k; k -= SCH.it[i].ops; } return -1; }

DI unsigned cvtpk(float lo, float hi) { f32x2 v = {lo, hi}; bf16x2 b = __builtin_convertvector(v, bf16x2); return __builtin_bit_cast(unsigned, b); }
DI f32x4 mfma16(bf16x8 a, bf16x8 b, f32x4 c) { return __builtin_amdgcn_mfma_f32_16x16x32_bf16(a, b, c, 0, 0, 0); }
DI bf16x8 trfrag(unsigned a0, unsigned a1) {
  bf16x4 lo = __builtin_amdgcn_ds_read_tr16_b64_v4bf16((LAS bf16x4*)(size_t)a0);
  bf16x4 hi = __builtin_amdgcn_ds_read_tr16_b64_v4bf16((LAS bf16x4*)(size_t)a1);
  return __builtin_shufflevector(lo, hi, 0, 1, 2, 3, 4, 5, 6, 7);
}
DI unsigned opq(unsigned a) { asm volatile("" : "+v"(a)); return a; }
DI bf16x8 ldsfrag(unsigned a) { return *(const LAS bf16x8*)(size_t)a; }
template <unsigned M0, int IMM, unsigned SO, bool NT> DI void dma1(unsigned wb, unsigned v, srd_t srd) {
  unsigned t;
  if constexpr (NT) asm volatile("s_add_u32 m0, %1, %4\n\ts_mov_b32 %0, %6\n\tbuffer_load_dwordx4 %2, %3, %0 offen offset:%5 nt lds"
                                 : "=&s"(t) : "s"(wb), "v"(v), "s"(srd), "i"(M0 - IMM), "i"(IMM), "i"(SO) : "memory", "scc");
  else              asm volatile("s_add_u32 m0, %1, %4\n\ts_mov_b32 %0, %6\n\tbuffer_load_dwordx4 %2, %3, %0 offen offset:%5 lds"
                                 : "=&s"(t) : "s"(wb), "v"(v), "s"(srd), "i"(M0 - IMM), "i"(IMM), "i"(SO) : "memory", "scc");
}

struct Ctx {
  srd_t sK, sV, sWq, sWo, sX, sWs;
  unsigned ldsb, wb, xb, sw;
  unsigned vK0, vK1, vV, vWq, vWo, vX;
  unsigned vTouch, vLse, dummy;
  u32x2 lse_g;
};

template <int I, int SUB> DI void issue_piece(Ctx& c) {
  constexpr Item it = SCH.it[I];
  if constexpr (it.type == IT_X) {
    dma1<SUB * 1024u, SUB * 128, 0u, false>(c.xb, c.vX, c.sX);
  } else if constexpr (it.type == IT_W) {
    constexpr unsigned m = slotW(it.idx, SUB >> 1) * 16384u + (SUB & 1) * 1024u;
    constexpr int imm = (SUB >> 1) * 512 + (SUB & 1) * 128;
    if constexpr (SUB < 3)
      asm volatile("s_add_u32 m0, %1, %4\n\ts_nop 0\n\tbuffer_load_dwordx4 %2, %3, %0 offen offset:%5 lds"
                   :: "s"(c.sw), "s"(c.wb), "v"(c.vWq), "s"(c.sWq), "i"(m - imm), "i"(imm) : "memory", "scc");
    else
      asm volatile("s_add_u32 m0, %1, %4\n\ts_nop 0\n\tbuffer_load_dwordx4 %2, %3, %0 offen offset:%5 lds\n\t"
                   "s_add_u32 %0, %0, 0x40000\n\ts_cmp_eq_u32 %0, 0x380000\n\ts_cselect_b32 %0, 0, %0"
                   : "+s"(c.sw) : "s"(c.wb), "v"(c.vWq), "s"(c.sWq), "i"(m - imm), "i"(imm) : "memory", "scc");
  } else if constexpr (it.type == IT_S) {
    constexpr unsigned m = slotS(it.idx) * 16384u + SUB * 1024u;
    if constexpr (it.idx < 16) dma1<m, SUB * 1024, it.idx * 16384u, true>(c.wb, SUB ? c.vK1 : c.vK0, c.sK);
    else dma1<m, SUB * 128, (it.idx - 16) * 16384u, true>(c.wb, c.vV, c.sV);
  } else if constexpr (it.type == IT_WO) {
    constexpr unsigned m = slotWO(it.idx) * 16384u + SUB * 1024u;
    constexpr unsigned so = (it.idx & 3) * (128u * 896u) + (it.idx >> 2) * 128u + SUB * (8u * 896u);
    dma1<m, 0, so, false>(c.wb, c.vWo, c.sWo);
  } else if constexpr (it.type == IT_TOUCH) {
    asm volatile("buffer_load_dword %0, %1, %2, 0 offen" : "+v"(c.dummy) : "v"(c.vTouch), "s"(c.sWo) : "memory");
  } else if constexpr (it.type == IT_LSE) {
    asm volatile("buffer_load_dwordx2 %0, %1, %2, 0 offen sc1" : "=v"(c.lse_g) : "v"(c.vLse), "s"(c.sWs) : "memory");
  }
}
template <int P, int G, int g, int K> DI void issue_one(Ctx& c) { if constexpr (K * G / npieces(P) == g) issue_piece<piece_item(P, K), piece_sub(P, K)>(c); }
template <int P, int G, int g, int... Ks> DI void issue_grp_(Ctx& c, iseq<Ks...>) { (issue_one<P, G, g, Ks>(c), ...); }
template <int P, int G, int g> DI void issue_grp(Ctx& c) { issue_grp_<P, G, g>(c, mkseq<npieces(P)>{}); }

#define FINE(P, K) do {} while (0)
template <int P> DI void step_sync(Ctx& c) {
  PIN();
  if constexpr (P == PT_LSE_USE) asm volatile("s_waitcnt vmcnt(%1) lgkmcnt(0)\n\ts_barrier" : "+v"(c.lse_g) : "i"(wait_cnt(P)) : "memory");
  else if constexpr (drains(P)) asm volatile("s_waitcnt vmcnt(%0) lgkmcnt(0)\n\ts_barrier" :: "i"(wait_cnt(P)) : "memory");
  else asm volatile("s_waitcnt vmcnt(%0)\n\ts_barrier" :: "i"(wait_cnt(P)) : "memory");
  PIN();
}

#define STAMP_AT(i) do {} while (0)

struct HCtx { f32x4 acc[2][8]; bf16x8 xf[2][14]; bf16x8 fr[2][8]; unsigned tb0, tb1; };
template <int T, int g> DI void head_grp(Ctx& c, HCtx& h, unsigned b0, unsigned b1) {
  if constexpr (T >= 1) {
    h.acc[0][g] = mfma16(h.fr[(T - 1) & 1][g], h.xf[0][T - 1], h.acc[0][g]);
    h.acc[1][g] = mfma16(h.fr[(T - 1) & 1][g], h.xf[1][T - 1], h.acc[1][g]);
  }
  if constexpr (T < 14) {
    if constexpr (g < 4) {
      h.fr[T & 1][2 * g]     = trfrag(b0 + g * 512u, b0 + g * 512u + 8192u);
      h.fr[T & 1][2 * g + 1] = trfrag(b1 + g * 512u, b1 + g * 512u + 8192u);
    }
    issue_grp<PT_H + T, 8, g>(c);
  }
  PIN();
}
template <int T, int... gs> DI void head_step(Ctx& c, HCtx& h, iseq<gs...>) {
  unsigned b0 = 0, b1 = 0;
  if constexpr (T < 14) {
    if constexpr (T > 0) step_sync<PT_H + T>(c);
    b0 = opq(h.tb0 + slotW(T, 0) * 16384u); b1 = opq(h.tb1 + slotW(T, 0) * 16384u);
  }
  (head_grp<T, gs>(c, h, b0, b1), ...);
}
template <int... Ts> DI void head_all(Ctx& c, HCtx& h, iseq<Ts...>) { (head_step<Ts>(c, h, mkseq<8>{}), ...); }

struct QCtx { f32x4 S[32]; bf16x8 qf[8]; bf16x8 fr[8]; unsigned kb[4], kk[4]; };
template <int F> DI void qk_frag(Ctx& c, QCtx& q) {
  constexpr int N = F >> 4, i = F & 15;
  if constexpr (F < 256 && i == 0) {
    if constexpr (N > 0) step_sync<PT_A + N>(c);
    #pragma unroll
    for (int u = 0; u < 4; ++u) q.kk[u] = opq(q.kb[u] + slotS(N) * 16384u);
  }
  if constexpr (F >= 8) {
    constexpr int M = F - 8, mt = 2 * (M >> 4) + (M & 1), mu = (M & 15) >> 1;
    if constexpr (mu == 0) q.S[mt] = mfma16(q.fr[M & 7], q.qf[mu], f32x4{0.f, 0.f, 0.f, 0.f});
    else q.S[mt] = mfma16(q.fr[M & 7], q.qf[mu], q.S[mt]);
  }
  if constexpr (F < 256) {
    constexpr int u = i >> 1, a = i & 1;
    q.fr[F & 7] = ldsfrag(q.kk[u & 3] + (u >> 2) * 256u + a * 8192u);
    if constexpr (npieces(PT_A + N) > 0 && (i & 1) == 0 && i < 8) issue_grp<PT_A + N, 4, (i >> 1)>(c);
  }
  PIN();
}
template <int... Fs> DI void qk_all(Ctx& c, QCtx& q, iseq<Fs...>) { (qk_frag<Fs>(c, q), ...); }

template <int Q> DI void sm_quarter(Ctx& c, f32x4 (&S)[32], bf16x8 (&pf)[16], float cs, float nm, float& l0, float& l1) {
  if constexpr (npieces(PT_SM + Q) > 0) issue_grp<PT_SM + Q, 1, 0>(c);
  #pragma unroll
  for (int t = 4 * Q; t < 4 * Q + 4; ++t) {
    float p[8];
    #pragma unroll
    for (int e = 0; e < 8; ++e) p[e] = __builtin_amdgcn_exp2f(__builtin_fmaf(S[2 * t + (e >> 2)][e & 3], cs, nm));
    l0 += (p[0] + p[1]) + (p[2] + p[3]); l1 += (p[4] + p[5]) + (p[6] + p[7]);
    u32x4 wv = {cvtpk(p[0], p[1]), cvtpk(p[2], p[3]), cvtpk(p[4], p[5]), cvtpk(p[6], p[7])};
    pf[t] = __builtin_bit_cast(bf16x8, wv);
  }
  PIN();
}

struct PCtx { f32x4 O[16]; bf16x8 pf[16]; bf16x8 fr[8]; unsigned tb0, tb1, b0, b1; };
template <int F> DI void pv_frag(Ctx& c, PCtx& p) {
  constexpr int N = F >> 4, db = F & 15;
  if constexpr (F < 256 && db == 0) {
    step_sync<PT_B + N>(c);
    p.b0 = opq(p.tb0 + slotS(16 + N) * 16384u); p.b1 = opq(p.tb1 + slotS(16 + N) * 16384u);
  }
  if constexpr (F >= 8) {
    constexpr int M = F - 8;
    if constexpr (M < 16) p.O[M & 15] = mfma16(p.fr[M & 7], p.pf[M >> 4], f32x4{0.f, 0.f, 0.f, 0.f});
    else p.O[M & 15] = mfma16(p.fr[M & 7], p.pf[M >> 4], p.O[M & 15]);
  }
  if constexpr (F < 256) {
    const unsigned a = ((db & 1) ? p.b1 : p.b0) + (db >> 1) * 512u;
    p.fr[F & 7] = trfrag(a, a + 8192u);
    if constexpr (npieces(PT_B + N) > 0 && (db & 1) == 0 && db < 8) issue_grp<PT_B + N, 4, (db >> 1)>(c);
  }
  PIN();
}
template <int... Fs> DI void pv_all(Ctx& c, PCtx& p, iseq<Fs...>) { (pv_frag<Fs>(c, p), ...); }

struct OCtx { f32x4 a0, a1; bf16x8 of[2][16]; bf16x8 fr[2][8]; unsigned wb, ob, pst, fl; u64 tag; };
template <int I, int g> DI void op_grp(Ctx& c, OCtx& o, unsigned w0, unsigned w1) {
  if constexpr (I >= 1) {
    constexpr int J = I - 1, U = 8 * (J & 1) + g;
    if constexpr (U == 0) { o.a0 = mfma16(o.fr[J & 1][g], o.of[0][U], f32x4{0.f, 0.f, 0.f, 0.f}); o.a1 = mfma16(o.fr[J & 1][g], o.of[1][U], f32x4{0.f, 0.f, 0.f, 0.f}); }
    else { o.a0 = mfma16(o.fr[J & 1][g], o.of[0][U], o.a0); o.a1 = mfma16(o.fr[J & 1][g], o.of[1][U], o.a1); }
  }
  if constexpr (I < 14) {
    if constexpr (g < 4) {
      const unsigned a = (g < 2 ? w0 : w1) + (g & 1) * 8192u;
      o.fr[I & 1][2 * g]     = trfrag(a, a + 2048u);
      o.fr[I & 1][2 * g + 1] = trfrag(a + 4096u, a + 6144u);
    }
    if constexpr (I == 0) {
      #pragma unroll
      for (int U = 2 * g; U < 2 * g + 2; ++U)
        #pragma unroll
        for (int z = 0; z < 2; ++z) o.of[z][U] = ldsfrag(o.ob + z * 8192u + (U >> 3) * 32768u + (U & 7) * 1024u);
    }
    if constexpr (npieces(PT_C + I) > 0) issue_grp<PT_C + I, 8, g>(c);
  }
  PIN();
}
template <int I, int... gs> DI void op_step(Ctx& c, OCtx& o, iseq<gs...>) {
  unsigned w0 = 0, w1 = 0;
  if constexpr (I < 14) {
    step_sync<PT_C + I>(c);
    w0 = opq(o.wb + slotWO(2 * I) * 16384u); w1 = opq(o.wb + slotWO(2 * I + 1) * 16384u);
  }
  (op_grp<I, gs>(c, o, w0, w1), ...);
  if constexpr (I >= 2 && (I & 1) == 0) {
    constexpr int ch = (I - 2) >> 1;
    unsigned x0 = cvtpk(o.a0[0], o.a0[1]), x1 = cvtpk(o.a0[2], o.a0[3]), y0 = cvtpk(o.a1[0], o.a1[1]), y1 = cvtpk(o.a1[2], o.a1[3]);
    auto r0 = __builtin_amdgcn_permlane16_swap(x0, y0, false, false);
    auto r1 = __builtin_amdgcn_permlane16_swap(x1, y1, false, false);
    u32x4 v = {r0[0], r1[0], r0[1], r1[1]};
    if constexpr (ch == 5) {
      asm volatile("s_waitcnt vmcnt(0)" ::: "memory");
      u32x2 t = {(unsigned)o.tag, (unsigned)(o.tag >> 32)};
      __builtin_amdgcn_raw_buffer_store_b64(t, c.sWs, o.fl, 0, 16);
    }
    __builtin_amdgcn_raw_buffer_store_b128(v, c.sWs, o.pst, ch * 8192u, 16);
  }
}
template <int... Is> DI void op_all(Ctx& c, OCtx& o, iseq<Is...>) { (op_step<Is>(c, o, mkseq<8>{}), ...); }

__global__ __launch_bounds__(512, 2)
void gqa2_fused(const char* __restrict__ xA, const char* __restrict__ kA, const char* __restrict__ vA, const char* __restrict__ wqA, const char* __restrict__ woA,
                const char* __restrict__ xB, const char* __restrict__ kB, const char* __restrict__ vB, const char* __restrict__ wqB, const char* __restrict__ woB,
                char* __restrict__ out, char* __restrict__ ws) {
  extern __shared__ __attribute__((aligned(16))) char lds[];
  const unsigned tid = threadIdx.x, l = tid & 63u;
  const unsigned w = __builtin_amdgcn_readfirstlane(tid >> 6);
  const unsigned bid = blockIdx.x, kvh = bid & 7u, j = bid >> 3, s = j & 1u, b = (j >> 1) & 7u, br = j >> 4;
  const unsigned grp = j >> 1, mem = kvh * 2u + s, wg = grp * 16u + mem;
  const unsigned q16 = l & 15u, g = l >> 4, hh = w >> 2;
  STAMP_AT(0);
  Ctx c;
  const unsigned rot = (j & 15u) >= 14u ? (j & 15u) - 9u : (j & 15u);
  c.ldsb = (unsigned)(size_t)lds; c.wb = c.ldsb + w * 2048u; c.sw = rot << 18; c.xb = c.ldsb + 6u * 16384u + w * 7168u;
  c.sX  = __builtin_amdgcn_make_buffer_rsrc((void*)((br ? xB : xA) + b * (64u * 896u)), 0, 64 * 896, 0x00020000);
  c.sWq = __builtin_amdgcn_make_buffer_rsrc((void*)((br ? wqB : wqA) + kvh * 1024u), 0, 448 * 8192 - kvh * 1024u, 0x00020000);
  const unsigned hi = l >> 5, pos = l & 31u, row = (l >> 2) & 7u, cc = (l & 3u) ^ ((row >> 2) << 1);
  const unsigned rg = w >> 1, col = hi * 64u + cc * 16u;
  c.vX  = (8u * w + (l >> 3)) * 896u + (((l & 7u) ^ (((w & 1u) << 2) | (l >> 4))) * 16u);
  c.vWq = (16u * (rg & 1u) + 4u * (rg >> 1) + 8u * (row >> 2) + (row & 3u)) * 8192u + (w & 1u) * 256u + col;
  issue_grp<0, 1, 0>(c);
  PIN();
  const size_t kvoff = ((size_t)(b * 8u + kvh) * 1024u + s * 512u) * 512u;
  c.sK  = __builtin_amdgcn_make_buffer_rsrc((void*)((br ? kB : kA) + kvoff), 0, 262144, 0x00020000);
  c.sV  = __builtin_amdgcn_make_buffer_rsrc((void*)((br ? vB : vA) + kvoff), 0, 262144, 0x00020000);
  c.sWo = __builtin_amdgcn_make_buffer_rsrc((void*)((br ? woB : woA) + kvh * (512u * 896u)), 0, 512 * 896, 0x00020000);
  c.sWs = __builtin_amdgcn_make_buffer_rsrc((void*)ws, 0, WS_END, 0x00020000);
  {
    const unsigned kin = hi * 512u + ((pos ^ hi) * 16u);
    c.vK0 = w * 2048u + (kin ^ (((2u * w) & 7u) * 32u));
    c.vK1 = w * 2048u + (kin ^ (((2u * w + 1u) & 7u) * 32u));
    c.vV  = (rg * 8u + row) * 512u + (w & 1u) * 256u + col;
    c.vWo = (16u * w + row) * 896u + col;
    c.vTouch = tid < 224u ? ((j & 15u) * 224u + tid) * 128u : 0x7fffffffu;
    c.vLse = WS_LSE + (wg ^ 1u) * 1024u + w * 128u + q16 * 8u;
    c.dummy = 0;
  }
  u64 epoch;
  asm volatile("s_load_dwordx2 %0, %1, %2\n\ts_waitcnt lgkmcnt(0)" : "=s"(epoch) : "s"(ws), "s"(grp * 256u) : "memory");
  const u64 tag = epoch + 0x9E3779B97F4A7C15ull;

  const unsigned tp = l & 3u, tr = (l >> 2) & 3u;
  const unsigned tin = (4u * (g & 1u) + tr) * 64u + tp * 8u;
  const unsigned tb0 = c.ldsb + (g >> 1) * 4096u + tin + (g & 1u) * 32u;
  const unsigned tb1 = c.ldsb + (g >> 1) * 4096u + tin + ((g & 1u) ^ 1u) * 32u;

  QCtx q;
  {
    HCtx h;
    const unsigned bb = w & 1u;
    h.tb0 = tb0 + hh * 16384u + bb * 2048u; h.tb1 = tb1 + hh * 16384u + bb * 2048u;
    #pragma unroll
    for (int i = 0; i < 8; ++i) { h.acc[0][i] = f32x4{0.f, 0.f, 0.f, 0.f}; h.acc[1][i] = f32x4{0.f, 0.f, 0.f, 0.f}; }
    step_sync<PT_H>(c);
    {
      const unsigned sw = q16 >> 1;
      unsigned t = rot;
      #pragma unroll
      for (int i = 0; i < 14; ++i) {
        const unsigned in = (t >> 1) * 1024u + (q16 & 7u) * 128u + (((((t & 1u) << 2) | g) ^ sw) * 16u);
        #pragma unroll
        for (int z = 0; z < 2; ++z) {
          const unsigned qz = (w & 3u) ^ z;
          h.xf[z][i] = ldsfrag(c.ldsb + 6u * 16384u + (2u * qz + (q16 >> 3)) * 7168u + in);
        }
        t = t == 13u ? 0u : t + 1u;
      }
    }
    PIN();
    head_all(c, h, mkseq<15>{});
    u32x4 own[4];
    #pragma unroll
    for (int z = 0; z < 2; ++z)
      #pragma unroll
      for (int u = 0; u < 4; ++u) {
        unsigned a0 = cvtpk(h.acc[z][2 * u][0], h.acc[z][2 * u][1]), a1 = cvtpk(h.acc[z][2 * u][2], h.acc[z][2 * u][3]);
        unsigned b0 = cvtpk(h.acc[z][2 * u + 1][0], h.acc[z][2 * u + 1][1]), b1 = cvtpk(h.acc[z][2 * u + 1][2], h.acc[z][2 * u + 1][3]);
        auto r0 = __builtin_amdgcn_permlane16_swap(a0, b0, false, false);
        auto r1 = __builtin_amdgcn_permlane16_swap(a1, b1, false, false);
        u32x4 v = {r0[0], r1[0], r0[1], r1[1]};
        if (z == 0) own[u] = v;
        else *(LAS u32x4*)(size_t)(c.ldsb + w * 4096u + u * 1024u + l * 16u) = v;
      }
    STAMP_AT(1);
    step_sync<PT_A>(c);
    #pragma unroll
    for (int u = 0; u < 4; ++u) {
      const u32x4 rv = *(const LAS u32x4*)(size_t)(c.ldsb + (w ^ 1u) * 4096u + u * 1024u + l * 16u);
      q.qf[u]     = __builtin_bit_cast(bf16x8, bb ? rv : own[u]);
      q.qf[4 + u] = __builtin_bit_cast(bf16x8, bb ? own[u] : rv);
    }
  }
  PCtx p;
  float lsum, lse;
  {
    const unsigned cm = ((g & 1u) << 1) | (g >> 1);
    #pragma unroll
    for (int u = 0; u < 4; ++u) q.kb[u] = c.ldsb + q16 * 512u + (((4u * u + cm) ^ q16) * 16u);
    qk_all(c, q, mkseq<264>{});
    asm volatile("" : "+v"(c.dummy));
    STAMP_AT(2);
    float m0 = q.S[0][0], m1 = q.S[0][1];
    #pragma unroll
    for (int i = 0; i < 32; ++i) { m0 = fmaxf(fmaxf(m0, q.S[i][0]), q.S[i][1]); m1 = fmaxf(fmaxf(m1, q.S[i][2]), q.S[i][3]); }
    float m = fmaxf(m0, m1);
    m = fmaxf(m, __shfl_xor(m, 16)); m = fmaxf(m, __shfl_xor(m, 32));
    const float cs = 0.0625f * 1.4426950408889634f;
    const float nm = -m * cs;
    float l0 = 0.f, l1 = 0.f;
    sm_quarter<0>(c, q.S, p.pf, cs, nm, l0, l1); sm_quarter<1>(c, q.S, p.pf, cs, nm, l0, l1);
    sm_quarter<2>(c, q.S, p.pf, cs, nm, l0, l1); sm_quarter<3>(c, q.S, p.pf, cs, nm, l0, l1);
    lsum = l0 + l1;
    lsum += __shfl_xor(lsum, 16); lsum += __shfl_xor(lsum, 32);
    lse = __builtin_amdgcn_logf(lsum) - nm;
    u32x2 gr = {__float_as_uint(lse), (unsigned)tag};
    __builtin_amdgcn_raw_buffer_store_b64(gr, c.sWs, WS_LSE + wg * 1024u + w * 128u + q16 * 8u, 0, 16);
  }
  STAMP_AT(3);
  OCtx o;
  {
    p.tb0 = tb0; p.tb1 = tb1;
    pv_all(c, p, mkseq<264>{});
    STAMP_AT(4);
    if (__builtin_expect(!__all(c.lse_g[1] == (unsigned)tag), 0)) {
      #pragma nounroll
      for (unsigned spins = 0; spins < (1u << 20); ++spins) {
        asm volatile("buffer_load_dwordx2 %0, %1, %2, 0 offen sc1\n\ts_waitcnt vmcnt(0)" : "=v"(c.lse_g) : "v"(c.vLse), "s"(c.sWs) : "memory");
        if (__all(c.lse_g[1] == (unsigned)tag)) break;
        __builtin_amdgcn_s_sleep(2);
      }
    }
    const float f = __builtin_amdgcn_rcpf(lsum * (1.f + __builtin_amdgcn_exp2f(__uint_as_float(c.lse_g[0]) - lse)));
    const unsigned ow = c.ldsb + w * 8192u + l * 16u;
    #pragma unroll
    for (int u = 0; u < 8; ++u) {
      u32x4 v = {cvtpk(p.O[2 * u][0] * f, p.O[2 * u][1] * f), cvtpk(p.O[2 * u][2] * f, p.O[2 * u][3] * f),
                 cvtpk(p.O[2 * u + 1][0] * f, p.O[2 * u + 1][1] * f), cvtpk(p.O[2 * u + 1][2] * f, p.O[2 * u + 1][3] * f)};
      *(LAS u32x4*)(size_t)(ow + u * 1024u) = v;
    }
  }
  {
    const unsigned nb = w & 3u;
    o.wb = c.ldsb + (g >> 1) * 1024u + (nb >> 1) * 512u + tin + (((nb & 1u) ^ (g & 1u)) * 32u);
    o.ob = c.ldsb + (w >> 2) * 16384u + l * 16u;
    o.pst = WS_PART + wg * (7u * 8192u) + w * 1024u + l * 16u;
    o.fl = l == 0 ? WS_FLAG + ((grp * 8u + w) * 2u) * 128u + mem * 8u : 0x7fffffffu;
    o.tag = tag;
    op_all(c, o, mkseq<15>{});
    asm volatile("s_waitcnt vmcnt(0)" ::: "memory");
    u32x2 t = {(unsigned)tag, (unsigned)(tag >> 32)};
    __builtin_amdgcn_raw_buffer_store_b64(t, c.sWs, l == 0 ? o.fl + 128u : 0x7fffffffu, 0, 16);
  }
  STAMP_AT(5);
  if (w < 7u) {
    const unsigned wsrc = mem >> 1;
    const unsigned fa = WS_FLAG + ((grp * 8u + wsrc) * 2u + (w >= 5u ? 1u : 0u)) * 128u + (l & 15u) * 8u;
    #pragma nounroll
    for (unsigned spins = 0; spins < (1u << 20); ++spins) {
      u32x2 fv;
      asm volatile("buffer_load_dwordx2 %0, %1, %2, 0 offen sc1\n\ts_waitcnt vmcnt(0)" : "=v"(fv) : "v"(fa), "s"(c.sWs) : "memory");
      if (__all(fv[0] == (unsigned)tag && fv[1] == (unsigned)(tag >> 32))) break;
      __builtin_amdgcn_s_sleep(1);
    }
    const unsigned pa = WS_PART + (grp * 16u + (l >> 5)) * (7u * 8192u) + w * 8192u + wsrc * 1024u + (mem & 1u) * 512u + (l & 31u) * 16u;
    u32x4 r[8];
    #pragma unroll
    for (int i = 0; i < 8; ++i) r[i] = __builtin_amdgcn_raw_buffer_load_b128(c.sWs, pa, i * (2u * 7u * 8192u), 16);
    float a[8];
    #pragma unroll
    for (int e = 0; e < 8; ++e) a[e] = 0.f;
    #pragma unroll
    for (int i = 0; i < 8; ++i)
      #pragma unroll
      for (int e = 0; e < 4; ++e) { a[2 * e] += __uint_as_float(r[i][e] << 16); a[2 * e + 1] += __uint_as_float(r[i][e] & 0xffff0000u); }
    #pragma unroll
    for (int e = 0; e < 8; ++e) a[e] += __shfl_xor(a[e], 32);
    if (l < 32u) {
      const unsigned ls = (mem & 1u) * 32u + l, gs = ls >> 4;
      const unsigned qq = 16u * (2u * (wsrc >> 2) + (gs & 1u)) + (ls & 15u);
      const unsigned n = 64u * w + 16u * (wsrc & 3u) + 8u * (gs >> 1);
      u32x4 v = {cvtpk(a[0], a[1]), cvtpk(a[2], a[3]), cvtpk(a[4], a[5]), cvtpk(a[6], a[7])};
      *(u32x4*)(out + (size_t)(b * 64u + qq) * 1792u + br * 896u + n * 2u) = v;
    }
    if (w == 6u && mem == 0u) {
      u32x2 t = {(unsigned)tag, (unsigned)(tag >> 32)};
      __builtin_amdgcn_raw_buffer_store_b64(t, c.sWs, l == 0 ? WS_EPOCH + grp * 256u : 0x7fffffffu, 0, 16);
    }
  }
  STAMP_AT(6);
}

extern "C" void kernel_launch(void* const* d_in, const int* in_sizes, int n_in, void* d_out, int out_size, void* d_ws, size_t ws_size, hipStream_t stream) {
  (void)in_sizes; (void)n_in; (void)out_size; (void)ws_size;
  (void)hipFuncSetAttribute((const void*)gqa2_fused, hipFuncAttributeMaxDynamicSharedMemorySize, 163840);
  hipLaunchKernelGGL(gqa2_fused, dim3(256), dim3(512), 163840, stream,
                     (const char*)d_in[0], (const char*)d_in[1], (const char*)d_in[2], (const char*)d_in[3], (const char*)d_in[4],
                     (const char*)d_in[5], (const char*)d_in[6], (const char*)d_in[7], (const char*)d_in[8], (const char*)d_in[9],
                     (char*)d_out, (char*)d_ws);
}
```

```cpp
#define STAMP 1
#include <hip/hip_runtime.h>
#include <utility>

typedef __bf16   bf16x8 __attribute__((ext_vector_type(8)));
typedef __bf16   bf16x4 __attribute__((ext_vector_type(4)));
typedef __bf16   bf16x2 __attribute__((ext_vector_type(2)));
typedef float    f32x4  __attribute__((ext_vector_type(4)));
typedef float    f32x2  __attribute__((ext_vector_type(2)));
typedef unsigned u32x4  __attribute__((ext_vector_type(4)));
typedef unsigned u32x2  __attribute__((ext_vector_type(2)));
typedef unsigned long long u64;
typedef __amdgpu_buffer_rsrc_t srd_t;
#define LAS __attribute__((address_space(3)))
#define DI __device__ __forceinline__
#define PIN() __builtin_amdgcn_sched_barrier(0)
template <int... Is> using iseq = std::integer_sequence<int, Is...>;
template <int N> using mkseq = std::make_integer_sequence<int, N>;

constexpr unsigned WS_EPOCH = 0;
constexpr unsigned WS_FLAG  = 4096;
constexpr unsigned WS_LSE   = WS_FLAG + 32768;
constexpr unsigned WS_PART  = 1u << 20;
constexpr unsigned WS_PART_BYTES = 256u * 7u * 8192u;
constexpr unsigned WS_END   = WS_PART + WS_PART_BYTES;
constexpr unsigned WS_STAMP = 32u << 20;
constexpr int NSTAMP = 7;

constexpr int PT_H = 1, PT_A = 15, PT_SM = 31, PT_B = 35, PT_C = 51, NPT = 65;
enum { IT_X = 0, IT_W = 1, IT_S = 2, IT_WO = 3, IT_TOUCH = 4, IT_LSE = 5 };
struct Item { int type, idx, pt, ops; };
struct Sched { Item it[96]; int n; };
constexpr int slotS(int n)  { constexpr int c[10] = {6, 7, 8, 9, 4, 5, 2, 3, 0, 1}; return c[n % 10]; }
constexpr int slotWO(int i) { constexpr int c[10] = {8, 9, 4, 5, 6, 7, 0, 1, 2, 3}; return c[i % 10]; }
constexpr int slotW(int t, int hh) { return 2 * (t % 3) + hh; }
constexpr int ptS[32] = {
    PT_H + 1, PT_H + 1, PT_H + 5, PT_H + 5, PT_H + 12, PT_H + 12, PT_A + 0,
    PT_A + 0, PT_A + 1, PT_A + 3, PT_A + 5, PT_A + 7, PT_A + 8, PT_A + 9, PT_A + 10, PT_A + 11,
    PT_A + 12, PT_A + 13, PT_A + 14, PT_A + 15, PT_SM + 1, PT_SM + 2, PT_SM + 3,
    PT_B + 0, PT_B + 1, PT_B + 3, PT_B + 5, PT_B + 7, PT_B + 9, PT_B + 10, PT_B + 11, PT_B + 12};
constexpr int ptWO[28] = {
    PT_B + 12, PT_B + 13, PT_B + 13, PT_B + 14, PT_B + 15, PT_C + 0,
    PT_C + 1, PT_C + 1, PT_C + 1, PT_C + 2, PT_C + 2, PT_C + 2, PT_C + 3, PT_C + 3, PT_C + 3, PT_C + 4, PT_C + 4, PT_C + 4,
    PT_C + 5, PT_C + 5, PT_C + 6, PT_C + 6, PT_C + 7, PT_C + 7, PT_C + 8, PT_C + 8, PT_C + 9, PT_C + 9};
constexpr int PT_TOUCH = PT_H + 8;
constexpr int PT_LSE_LD = PT_B + 8, PT_LSE_USE = PT_B + 12;
constexpr int ptW(int t) { return t < 3 ? 0 : PT_H + t - 2; }
constexpr bool drains(int p) { return p <= PT_A || p >= PT_B + 10; }

constexpr Sched make_sched() {
  Sched s{}; int n = 0;
  for (int p = 0; p < NPT; ++p) {
    if (p == 0) {
      s.it[n++] = Item{IT_X, 0, 0, 7};
      s.it[n++] = Item{IT_W, 0, 0, 4};
      s.it[n++] = Item{IT_W, 1, 0, 4};
      s.it[n++] = Item{IT_W, 2, 0, 4};
      continue;
    }
    for (int t = 3; t < 14; ++t) if (ptW(t) == p) s.it[n++] = Item{IT_W, t, p, 4};
    for (int i = 0; i < 32; ++i) if (ptS[i] == p) s.it[n++] = Item{IT_S, i, p, 2};
    for (int i = 0; i < 28; ++i) if (ptWO[i] == p) s.it[n++] = Item{IT_WO, i, p, 2};
    if (p == PT_TOUCH) s.it[n++] = Item{IT_TOUCH, 0, p, 1};
    if (p == PT_LSE_LD) s.it[n++] = Item{IT_LSE, 0, p, 1};
  }
  s.n = n; return s;
}
constexpr Sched SCH = make_sched();
constexpr int find_item(int type, int idx) { for (int i = 0; i < SCH.n; ++i) if (SCH.it[i].type == type && SCH.it[i].idx == idx) return i; return -1000; }
constexpr int ops_after(int L, int p) { int c = 0; for (int i = L + 1; i < SCH.n; ++i) if (SCH.it[i].pt < p) c += SCH.it[i].ops; return c; }
constexpr int imax(int a, int b) { return a > b ? a : b; }
constexpr int need_item(int p) {
  int L = -1000;
  if (p >= PT_H && p < PT_A) L = find_item(IT_W, p - PT_H);
  else if (p >= PT_A && p < PT_SM) L = find_item(IT_S, p - PT_A);
  else if (p >= PT_B && p < PT_C) L = find_item(IT_S, 16 + p - PT_B);
  else if (p >= PT_C) L = imax(find_item(IT_WO, 2 * (p - PT_C)), find_item(IT_WO, 2 * (p - PT_C) + 1));
  if (p == PT_LSE_USE) L = imax(L, find_item(IT_LSE, 0));
  return L;
}
constexpr int wait_cnt(int p) { const int c = ops_after(need_item(p), p); return c > 63 ? 63 : c; }
constexpr bool is_bar(int p) { return p >= 1 && !(p >= PT_SM && p < PT_B); }
constexpr int free_at(int use) {
  int p = use + 1; while (p < NPT && !is_bar(p)) ++p;
  if (p < NPT && (drains(p) || p != use + 1)) return p;
  ++p; while (p < NPT && !is_bar(p)) ++p;
  return p;
}
constexpr bool sched_ok() {
  for (int p = 1; p < NPT; ++p) { if (!is_bar(p)) continue; int L = need_item(p); if (L < 0 || SCH.it[L].pt >= p) return false; }
  int fr[10] = {0, 0, 0, 0, 0, 0, PT_H + 1, PT_H + 1, PT_H + 1, PT_H + 1};
  for (int i = 0; i < SCH.n; ++i) {
    const Item& I = SCH.it[i]; int sl[2] = {-1, -1}; int use = 0;
    if (I.type == IT_W) { sl[0] = slotW(I.idx, 0); sl[1] = slotW(I.idx, 1); use = PT_H + I.idx; }
    else if (I.type == IT_S) { sl[0] = slotS(I.idx); use = I.idx < 16 ? PT_A + I.idx : PT_B + I.idx - 16;
      if (sl[0] < 2 && I.pt <= PT_A) return false;
      if (sl[0] < 4 && free_at(use) > PT_B + 15) return false; }
    else if (I.type == IT_WO) { sl[0] = slotWO(I.idx); use = PT_C + I.idx / 2; if (sl[0] < 4 && I.pt <= PT_C) return false; }
    for (int k = 0; k < 2; ++k) if (sl[k] >= 0) { if (I.pt < fr[sl[k]]) return false; fr[sl[k]] = free_at(use); }
  }
  if (slotW(12, 0) != 0) return false;
  return true;
}
static_assert(sched_ok(), "schedule");
constexpr int npieces(int p) { int c = 0; for (int i = 0; i < SCH.n; ++i) if (SCH.it[i].pt == p) c += SCH.it[i].ops; return c; }
constexpr int piece_item(int p, int k) { for (int i = 0; i < SCH.n; ++i) if (SCH.it[i].pt == p) { if (k < SCH.it[i].ops) return i; k -= SCH.it[i].ops; } return -1; }
constexpr int piece_sub(int p, int k) { for (int i = 0; i < SCH.n; ++i) if (SCH.it[i].pt == p) { if (k < SCH.it[i].ops) return k; k -= SCH.it[i].ops; } return -1; }

DI unsigned cvtpk(float lo, float hi) { f32x2 v = {lo, hi}; bf16x2 b = __builtin_convertvector(v, bf16x2); return __builtin_bit_cast(unsigned, b); }
DI f32x4 mfma16(bf16x8 a, bf16x8 b, f32x4 c) { return __builtin_amdgcn_mfma_f32_16x16x32_bf16(a, b, c, 0, 0, 0); }
DI bf16x8 trfrag(unsigned a0, unsigned a1) {
  bf16x4 lo = __builtin_amdgcn_ds_read_tr16_b64_v4bf16((LAS bf16x4*)(size_t)a0);
  bf16x4 hi = __builtin_amdgcn_ds_read_tr16_b64_v4bf16((LAS bf16x4*)(size_t)a1);
  return __builtin_shufflevector(lo, hi, 0, 1, 2, 3, 4, 5, 6, 7);
}
DI unsigned opq(unsigned a) { asm volatile("" : "+v"(a)); return a; }
DI bf16x8 ldsfrag(unsigned a) { return *(const LAS bf16x8*)(size_t)a; }
template <unsigned M0, int IMM, unsigned SO, bool NT> DI void dma1(unsigned wb, unsigned v, srd_t srd) {
  unsigned t;
  if constexpr (NT) asm volatile("s_add_u32 m0, %1, %4\n\ts_mov_b32 %0, %6\n\tbuffer_load_dwordx4 %2, %3, %0 offen offset:%5 nt lds"
                                 : "=&s"(t) : "s"(wb), "v"(v), "s"(srd), "i"(M0 - IMM), "i"(IMM), "i"(SO) : "memory", "scc");
  else              asm volatile("s_add_u32 m0, %1, %4\n\ts_mov_b32 %0, %6\n\tbuffer_load_dwordx4 %2, %3, %0 offen offset:%5 lds"
                                 : "=&s"(t) : "s"(wb), "v"(v), "s"(srd), "i"(M0 - IMM), "i"(IMM), "i"(SO) : "memory", "scc");
}

struct Ctx {
  srd_t sK, sV, sWq, sWo, sX, sWs;
  unsigned ldsb, wb, xb, sw;
  unsigned vK0, vK1, vV, vWq, vWo, vX;
  unsigned vTouch, vLse, dummy;
  u32x2 lse_g;
};

template <int I, int SUB> DI void issue_piece(Ctx& c) {
  constexpr Item it = SCH.it[I];
  if constexpr (it.type == IT_X) {
    dma1<SUB * 1024u, SUB * 128, 0u, false>(c.xb, c.vX, c.sX);
  } else if constexpr (it.type == IT_W) {
    constexpr unsigned m = slotW(it.idx, SUB >> 1) * 16384u + (SUB & 1) * 1024u;
    constexpr int imm = (SUB >> 1) * 512 + (SUB & 1) * 128;
    if constexpr (SUB < 3)
      asm volatile("s_add_u32 m0, %1, %4\n\ts_nop 0\n\tbuffer_load_dwordx4 %2, %3, %0 offen offset:%5 lds"
                   :: "s"(c.sw), "s"(c.wb), "v"(c.vWq), "s"(c.sWq), "i"(m - imm), "i"(imm) : "memory", "scc");
    else
      asm volatile("s_add_u32 m0, %1, %4\n\ts_nop 0\n\tbuffer_load_dwordx4 %2, %3, %0 offen offset:%5 lds\n\t"
                   "s_add_u32 %0, %0, 0x40000\n\ts_cmp_eq_u32 %0, 0x380000\n\ts_cselect_b32 %0, 0, %0"
                   : "+s"(c.sw) : "s"(c.wb), "v"(c.vWq), "s"(c.sWq), "i"(m - imm), "i"(imm) : "memory", "scc");
  } else if constexpr (it.type == IT_S) {
    constexpr unsigned m = slotS(it.idx) * 16384u + SUB * 1024u;
    if constexpr (it.idx < 16) dma1<m, SUB * 1024, it.idx * 16384u, true>(c.wb, SUB ? c.vK1 : c.vK0, c.sK);
    else dma1<m, SUB * 128, (it.idx - 16) * 16384u, true>(c.wb, c.vV, c.sV);
  } else if constexpr (it.type == IT_WO) {
    constexpr unsigned m = slotWO(it.idx) * 16384u + SUB * 1024u;
    constexpr unsigned so = (it.idx & 3) * (128u * 896u) + (it.idx >> 2) * 128u + SUB * (8u * 896u);
    dma1<m, 0, so, false>(c.wb, c.vWo, c.sWo);
  } else if constexpr (it.type == IT_TOUCH) {
    asm volatile("buffer_load_dword %0, %1, %2, 0 offen" : "+v"(c.dummy) : "v"(c.vTouch), "s"(c.sWo) : "memory");
  } else if constexpr (it.type == IT_LSE) {
    asm volatile("buffer_load_dwordx2 %0, %1, %2, 0 offen sc1" : "=v"(c.lse_g) : "v"(c.vLse), "s"(c.sWs) : "memory");
  }
}
template <int P, int G, int g, int K> DI void issue_one(Ctx& c) { if constexpr (K * G / npieces(P) == g) issue_piece<piece_item(P, K), piece_sub(P, K)>(c); }
template <int P, int G, int g, int... Ks> DI void issue_grp_(Ctx& c, iseq<Ks...>) { (issue_one<P, G, g, Ks>(c), ...); }
template <int P, int G, int g> DI void issue_grp(Ctx& c) { issue_grp_<P, G, g>(c, mkseq<npieces(P)>{}); }

#define FINE(P, K) do {} while (0)
template <int P> DI void step_sync(Ctx& c) {
  PIN();
  if constexpr (P == PT_LSE_USE) asm volatile("s_waitcnt vmcnt(%1) lgkmcnt(0)\n\ts_barrier" : "+v"(c.lse_g) : "i"(wait_cnt(P)) : "memory");
  else if constexpr (drains(P)) asm volatile("s_waitcnt vmcnt(%0) lgkmcnt(0)\n\ts_barrier" :: "i"(wait_cnt(P)) : "memory");
  else asm volatile("s_waitcnt vmcnt(%0)\n\ts_barrier" :: "i"(wait_cnt(P)) : "memory");
  PIN();
}

#define STAMP_AT(i) do { if (threadIdx.x == 0) { stamps[i] = __builtin_amdgcn_s_memrealtime(); stamps[8 + i] = __builtin_amdgcn_s_memtime(); } } while (0)

struct HCtx { f32x4 acc[2][8]; bf16x8 xf[2][14]; bf16x8 fr[2][8]; unsigned tb0, tb1; };
template <int T, int g> DI void head_grp(Ctx& c, HCtx& h, unsigned b0, unsigned b1) {
  if constexpr (T >= 1) {
    h.acc[0][g] = mfma16(h.fr[(T - 1) & 1][g], h.xf[0][T - 1], h.acc[0][g]);
    h.acc[1][g] = mfma16(h.fr[(T - 1) & 1][g], h.xf[1][T - 1], h.acc[1][g]);
  }
  if constexpr (T < 14) {
    if constexpr (g < 4) {
      h.fr[T & 1][2 * g]     = trfrag(b0 + g * 512u, b0 + g * 512u + 8192u);
      h.fr[T & 1][2 * g + 1] = trfrag(b1 + g * 512u, b1 + g * 512u + 8192u);
    }
    issue_grp<PT_H + T, 8, g>(c);
  }
  PIN();
}
template <int T, int... gs> DI void head_step(Ctx& c, HCtx& h, iseq<gs...>) {
  unsigned b0 = 0, b1 = 0;
  if constexpr (T < 14) {
    if constexpr (T > 0) step_sync<PT_H + T>(c);
    b0 = opq(h.tb0 + slotW(T, 0) * 16384u); b1 = opq(h.tb1 + slotW(T, 0) * 16384u);
  }
  (head_grp<T, gs>(c, h, b0, b1), ...);
}
template <int... Ts> DI void head_all(Ctx& c, HCtx& h, iseq<Ts...>) { (head_step<Ts>(c, h, mkseq<8>{}), ...); }

struct QCtx { f32x4 S[32]; bf16x8 qf[8]; bf16x8 fr[8]; unsigned kb[4], kk[4]; };
template <int F> DI void qk_frag(Ctx& c, QCtx& q) {
  constexpr int N = F >> 4, i = F & 15;
  if constexpr (F < 256 && i == 0) {
    if constexpr (N > 0) step_sync<PT_A + N>(c);
    #pragma unroll
    for (int u = 0; u < 4; ++u) q.kk[u] = opq(q.kb[u] + slotS(N) * 16384u);
  }
  if constexpr (F >= 8) {
    constexpr int M = F - 8, mt = 2 * (M >> 4) + (M & 1), mu = (M & 15) >> 1;
    if constexpr (mu == 0) q.S[mt] = mfma16(q.fr[M & 7], q.qf[mu], f32x4{0.f, 0.f, 0.f, 0.f});
    else q.S[mt] = mfma16(q.fr[M & 7], q.qf[mu], q.S[mt]);
  }
  if constexpr (F < 256) {
    constexpr int u = i >> 1, a = i & 1;
    q.fr[F & 7] = ldsfrag(q.kk[u & 3] + (u >> 2) * 256u + a * 8192u);
    if constexpr (npieces(PT_A + N) > 0 && (i & 1) == 0 && i < 8) issue_grp<PT_A + N, 4, (i >> 1)>(c);
  }
  PIN();
}
template <int... Fs> DI void qk_all(Ctx& c, QCtx& q, iseq<Fs...>) { (qk_frag<Fs>(c, q), ...); }

template <int Q> DI void sm_quarter(Ctx& c, f32x4 (&S)[32], bf16x8 (&pf)[16], float cs, float nm, float& l0, float& l1) {
  if constexpr (npieces(PT_SM + Q) > 0) issue_grp<PT_SM + Q, 1, 0>(c);
  #pragma unroll
  for (int t = 4 * Q; t < 4 * Q + 4; ++t) {
    float p[8];
    #pragma unroll
    for (int e = 0; e < 8; ++e) p[e] = __builtin_amdgcn_exp2f(__builtin_fmaf(S[2 * t + (e >> 2)][e & 3], cs, nm));
    l0 += (p[0] + p[1]) + (p[2] + p[3]); l1 += (p[4] + p[5]) + (p[6] + p[7]);
    u32x4 wv = {cvtpk(p[0], p[1]), cvtpk(p[2], p[3]), cvtpk(p[4], p[5]), cvtpk(p[6], p[7])};
    pf[t] = __builtin_bit_cast(bf16x8, wv);
  }
  PIN();
}

struct PCtx { f32x4 O[16]; bf16x8 pf[16]; bf16x8 fr[8]; unsigned tb0, tb1, b0, b1; };
template <int F> DI void pv_frag(Ctx& c, PCtx& p) {
  constexpr int N = F >> 4, db = F & 15;
  if constexpr (F < 256 && db == 0) {
    step_sync<PT_B + N>(c);
    p.b0 = opq(p.tb0 + slotS(16 + N) * 16384u); p.b1 = opq(p.tb1 + slotS(16 + N) * 16384u);
  }
  if constexpr (F >= 8) {
    constexpr int M = F - 8;
    if constexpr (M < 16) p.O[M & 15] = mfma16(p.fr[M & 7], p.pf[M >> 4], f32x4{0.f, 0.f, 0.f, 0.f});
    else p.O[M & 15] = mfma16(p.fr[M & 7], p.pf[M >> 4], p.O[M & 15]);
  }
  if constexpr (F < 256) {
    const unsigned a = ((db & 1) ? p.b1 : p.b0) + (db >> 1) * 512u;
    p.fr[F & 7] = trfrag(a, a + 8192u);
    if constexpr (npieces(PT_B + N) > 0 && (db & 1) == 0 && db < 8) issue_grp<PT_B + N, 4, (db >> 1)>(c);
  }
  PIN();
}
template <int... Fs> DI void pv_all(Ctx& c, PCtx& p, iseq<Fs...>) { (pv_frag<Fs>(c, p), ...); }

struct OCtx { f32x4 a0, a1; bf16x8 of[2][16]; bf16x8 fr[2][8]; unsigned wb, ob, pst, fl; u64 tag; };
template <int I, int g> DI void op_grp(Ctx& c, OCtx& o, unsigned w0, unsigned w1) {
  if constexpr (I >= 1) {
    constexpr int J = I - 1, U = 8 * (J & 1) + g;
    if constexpr (U == 0) { o.a0 = mfma16(o.fr[J & 1][g], o.of[0][U], f32x4{0.f, 0.f, 0.f, 0.f}); o.a1 = mfma16(o.fr[J & 1][g], o.of[1][U], f32x4{0.f, 0.f, 0.f, 0.f}); }
    else { o.a0 = mfma16(o.fr[J & 1][g], o.of[0][U], o.a0); o.a1 = mfma16(o.fr[J & 1][g], o.of[1][U], o.a1); }
  }
  if constexpr (I < 14) {
    if constexpr (g < 4) {
      const unsigned a = (g < 2 ? w0 : w1) + (g & 1) * 8192u;
      o.fr[I & 1][2 * g]     = trfrag(a, a + 2048u);
      o.fr[I & 1][2 * g + 1] = trfrag(a + 4096u, a + 6144u);
    }
    if constexpr (I == 0) {
      #pragma unroll
      for (int U = 2 * g; U < 2 * g + 2; ++U)
        #pragma unroll
        for (int z = 0; z < 2; ++z) o.of[z][U] = ldsfrag(o.ob + z * 8192u + (U >> 3) * 32768u + (U & 7) * 1024u);
    }
    if constexpr (npieces(PT_C + I) > 0) issue_grp<PT_C + I, 8, g>(c);
  }
  PIN();
}
template <int I, int... gs> DI void op_step(Ctx& c, OCtx& o, iseq<gs...>) {
  unsigned w0 = 0, w1 = 0;
  if constexpr (I < 14) {
    step_sync<PT_C + I>(c);
    w0 = opq(o.wb + slotWO(2 * I) * 16384u); w1 = opq(o.wb + slotWO(2 * I + 1) * 16384u);
  }
  (op_grp<I, gs>(c, o, w0, w1), ...);
  if constexpr (I >= 2 && (I & 1) == 0) {
    constexpr int ch = (I - 2) >> 1;
    unsigned x0 = cvtpk(o.a0[0], o.a0[1]), x1 = cvtpk(o.a0[2], o.a0[3]), y0 = cvtpk(o.a1[0], o.a1[1]), y1 = cvtpk(o.a1[2], o.a1[3]);
    auto r0 = __builtin_amdgcn_permlane16_swap(x0, y0, false, false);
    auto r1 = __builtin_amdgcn_permlane16_swap(x1, y1, false, false);
    u32x4 v = {r0[0], r1[0], r0[1], r1[1]};
    if constexpr (ch == 5) {
      asm volatile("s_waitcnt vmcnt(0)" ::: "memory");
      u32x2 t = {(unsigned)o.tag, (unsigned)(o.tag >> 32)};
      __builtin_amdgcn_raw_buffer_store_b64(t, c.sWs, o.fl, 0, 16);
    }
    __builtin_amdgcn_raw_buffer_store_b128(v, c.sWs, o.pst, ch * 8192u, 16);
  }
}
template <int... Is> DI void op_all(Ctx& c, OCtx& o, iseq<Is...>) { (op_step<Is>(c, o, mkseq<8>{}), ...); }

__global__ __launch_bounds__(512, 2)
void gqa2_fused(const char* __restrict__ xA, const char* __restrict__ kA, const char* __restrict__ vA, const char* __restrict__ wqA, const char* __restrict__ woA,
                const char* __restrict__ xB, const char* __restrict__ kB, const char* __restrict__ vB, const char* __restrict__ wqB, const char* __restrict__ woB,
                char* __restrict__ out, char* __restrict__ ws) {
  extern __shared__ __attribute__((aligned(16))) char lds[];
  const unsigned tid = threadIdx.x, l = tid & 63u;
  const unsigned w = __builtin_amdgcn_readfirstlane(tid >> 6);
  const unsigned bid = blockIdx.x, kvh = bid & 7u, j = bid >> 3, s = j & 1u, b = (j >> 1) & 7u, br = j >> 4;
  const unsigned grp = j >> 1, mem = kvh * 2u + s, wg = grp * 16u + mem;
  const unsigned q16 = l & 15u, g = l >> 4, hh = w >> 2;
  u64* stamps = (u64*)(ws + WS_STAMP) + bid * 16;
  STAMP_AT(0);
  Ctx c;
  const unsigned rot = (j & 15u) >= 14u ? (j & 15u) - 9u : (j & 15u);
  c.ldsb = (unsigned)(size_t)lds; c.wb = c.ldsb + w * 2048u; c.sw = rot << 18; c.xb = c.ldsb + 6u * 16384u + w * 7168u;
  c.sX  = __builtin_amdgcn_make_buffer_rsrc((void*)((br ? xB : xA) + b * (64u * 896u)), 0, 64 * 896, 0x00020000);
  c.sWq = __builtin_amdgcn_make_buffer_rsrc((void*)((br ? wqB : wqA) + kvh * 1024u), 0, 448 * 8192 - kvh * 1024u, 0x00020000);
  const unsigned hi = l >> 5, pos = l & 31u, row = (l >> 2) & 7u, cc = (l & 3u) ^ ((row >> 2) << 1);
  const unsigned rg = w >> 1, col = hi * 64u + cc * 16u;
  c.vX  = (8u * w + (l >> 3)) * 896u + (((l & 7u) ^ (((w & 1u) << 2) | (l >> 4))) * 16u);
  c.vWq = (16u * (rg & 1u) + 4u * (rg >> 1) + 8u * (row >> 2) + (row & 3u)) * 8192u + (w & 1u) * 256u + col;
  issue_grp<0, 1, 0>(c);
  PIN();
  const size_t kvoff = ((size_t)(b * 8u + kvh) * 1024u + s * 512u) * 512u;
  c.sK  = __builtin_amdgcn_make_buffer_rsrc((void*)((br ? kB : kA) + kvoff), 0, 262144, 0x00020000);
  c.sV  = __builtin_amdgcn_make_buffer_rsrc((void*)((br ? vB : vA) + kvoff), 0, 262144, 0x00020000);
  c.sWo = __builtin_amdgcn_make_buffer_rsrc((void*)((br ? woB : woA) + kvh * (512u * 896u)), 0, 512 * 896, 0x00020000);
  c.sWs = __builtin_amdgcn_make_buffer_rsrc((void*)ws, 0, WS_END, 0x00020000);
  {
    const unsigned kin = hi * 512u + ((pos ^ hi) * 16u);
    c.vK0 = w * 2048u + (kin ^ (((2u * w) & 7u) * 32u));
    c.vK1 = w * 2048u + (kin ^ (((2u * w + 1u) & 7u) * 32u));
    c.vV  = (rg * 8u + row) * 512u + (w & 1u) * 256u + col;
    c.vWo = (16u * w + row) * 896u + col;
    c.vTouch = tid < 224u ? ((j & 15u) * 224u + tid) * 128u : 0x7fffffffu;
    c.vLse = WS_LSE + (wg ^ 1u) * 1024u + w * 128u + q16 * 8u;
    c.dummy = 0;
  }
  u64 epoch;
  asm volatile("s_load_dwordx2 %0, %1, %2\n\ts_waitcnt lgkmcnt(0)" : "=s"(epoch) : "s"(ws), "s"(grp * 256u) : "memory");
  const u64 tag = epoch + 0x9E3779B97F4A7C15ull;

  const unsigned tp = l & 3u, tr = (l >> 2) & 3u;
  const unsigned tin = (4u * (g & 1u) + tr) * 64u + tp * 8u;
  const unsigned tb0 = c.ldsb + (g >> 1) * 4096u + tin + (g & 1u) * 32u;
  const unsigned tb1 = c.ldsb + (g >> 1) * 4096u + tin + ((g & 1u) ^ 1u) * 32u;

  QCtx q;
  {
    HCtx h;
    const unsigned bb = w & 1u;
    h.tb0 = tb0 + hh * 16384u + bb * 2048u; h.tb1 = tb1 + hh * 16384u + bb * 2048u;
    #pragma unroll
    for (int i = 0; i < 8; ++i) { h.acc[0][i] = f32x4{0.f, 0.f, 0.f, 0.f}; h.acc[1][i] = f32x4{0.f, 0.f, 0.f, 0.f}; }
    step_sync<PT_H>(c);
    {
      const unsigned sw = q16 >> 1;
      unsigned t = rot;
      #pragma unroll
      for (int i = 0; i < 14; ++i) {
        const unsigned in = (t >> 1) * 1024u + (q16 & 7u) * 128u + (((((t & 1u) << 2) | g) ^ sw) * 16u);
        #pragma unroll
        for (int z = 0; z < 2; ++z) {
          const unsigned qz = (w & 3u) ^ z;
          h.xf[z][i] = ldsfrag(c.ldsb + 6u * 16384u + (2u * qz + (q16 >> 3)) * 7168u + in);
        }
        t = t == 13u ? 0u : t + 1u;
      }
    }
    PIN();
    head_all(c, h, mkseq<15>{});
    u32x4 own[4];
    #pragma unroll
    for (int z = 0; z < 2; ++z)
      #pragma unroll
      for (int u = 0; u < 4; ++u) {
        unsigned a0 = cvtpk(h.acc[z][2 * u][0], h.acc[z][2 * u][1]), a1 = cvtpk(h.acc[z][2 * u][2], h.acc[z][2 * u][3]);
        unsigned b0 = cvtpk(h.acc[z][2 * u + 1][0], h.acc[z][2 * u + 1][1]), b1 = cvtpk(h.acc[z][2 * u + 1][2], h.acc[z][2 * u + 1][3]);
        auto r0 = __builtin_amdgcn_permlane16_swap(a0, b0, false, false);
        auto r1 = __builtin_amdgcn_permlane16_swap(a1, b1, false, false);
        u32x4 v = {r0[0], r1[0], r0[1], r1[1]};
        if (z == 0) own[u] = v;
        else *(LAS u32x4*)(size_t)(c.ldsb + w * 4096u + u * 1024u + l * 16u) = v;
      }
    STAMP_AT(1);
    step_sync<PT_A>(c);
    #pragma unroll
    for (int u = 0; u < 4; ++u) {
      const u32x4 rv = *(const LAS u32x4*)(size_t)(c.ldsb + (w ^ 1u) * 4096u + u * 1024u + l * 16u);
      q.qf[u]     = __builtin_bit_cast(bf16x8, bb ? rv : own[u]);
      q.qf[4 + u] = __builtin_bit_cast(bf16x8, bb ? own[u] : rv);
    }
  }
  PCtx p;
  float lsum, lse;
  {
    const unsigned cm = ((g & 1u) << 1) | (g >> 1);
    #pragma unroll
    for (int u = 0; u < 4; ++u) q.kb[u] = c.ldsb + q16 * 512u + (((4u * u + cm) ^ q16) * 16u);
    qk_all(c, q, mkseq<264>{});
    asm volatile("" : "+v"(c.dummy));
    STAMP_AT(2);
    float m0 = q.S[0][0], m1 = q.S[0][1];
    #pragma unroll
    for (int i = 0; i < 32; ++i) { m0 = fmaxf(fmaxf(m0, q.S[i][0]), q.S[i][1]); m1 = fmaxf(fmaxf(m1, q.S[i][2]), q.S[i][3]); }
    float m = fmaxf(m0, m1);
    m = fmaxf(m, __shfl_xor(m, 16)); m = fmaxf(m, __shfl_xor(m, 32));
    const float cs = 0.0625f * 1.4426950408889634f;
    const float nm = -m * cs;
    float l0 = 0.f, l1 = 0.f;
    sm_quarter<0>(c, q.S, p.pf, cs, nm, l0, l1); sm_quarter<1>(c, q.S, p.pf, cs, nm, l0, l1);
    sm_quarter<2>(c, q.S, p.pf, cs, nm, l0, l1); sm_quarter<3>(c, q.S, p.pf, cs, nm, l0, l1);
    lsum = l0 + l1;
    lsum += __shfl_xor(lsum, 16); lsum += __shfl_xor(lsum, 32);
    lse = __builtin_amdgcn_logf(lsum) - nm;
    u32x2 gr = {__float_as_uint(lse), (unsigned)tag};
    __builtin_amdgcn_raw_buffer_store_b64(gr, c.sWs, WS_LSE + wg * 1024u + w * 128u + q16 * 8u, 0, 16);
  }
  STAMP_AT(3);
  OCtx o;
  {
    p.tb0 = tb0; p.tb1 = tb1;
    pv_all(c, p, mkseq<264>{});
    STAMP_AT(4);
    if (__builtin_expect(!__all(c.lse_g[1] == (unsigned)tag), 0)) {
      #pragma nounroll
      for (unsigned spins = 0; spins < (1u << 20); ++spins) {
        asm volatile("buffer_load_dwordx2 %0, %1, %2, 0 offen sc1\n\ts_waitcnt vmcnt(0)" : "=v"(c.lse_g) : "v"(c.vLse), "s"(c.sWs) : "memory");
        if (__all(c.lse_g[1] == (unsigned)tag)) break;
        __builtin_amdgcn_s_sleep(2);
      }
    }
    const float f = __builtin_amdgcn_rcpf(lsum * (1.f + __builtin_amdgcn_exp2f(__uint_as_float(c.lse_g[0]) - lse)));
    const unsigned ow = c.ldsb + w * 8192u + l * 16u;
    #pragma unroll
    for (int u = 0; u < 8; ++u) {
      u32x4 v = {cvtpk(p.O[2 * u][0] * f, p.O[2 * u][1] * f), cvtpk(p.O[2 * u][2] * f, p.O[2 * u][3] * f),
                 cvtpk(p.O[2 * u + 1][0] * f, p.O[2 * u + 1][1] * f), cvtpk(p.O[2 * u + 1][2] * f, p.O[2 * u + 1][3] * f)};
      *(LAS u32x4*)(size_t)(ow + u * 1024u) = v;
    }
  }
  {
    const unsigned nb = w & 3u;
    o.wb = c.ldsb + (g >> 1) * 1024u + (nb >> 1) * 512u + tin + (((nb & 1u) ^ (g & 1u)) * 32u);
    o.ob = c.ldsb + (w >> 2) * 16384u + l * 16u;
    o.pst = WS_PART + wg * (7u * 8192u) + w * 1024u + l * 16u;
    o.fl = l == 0 ? WS_FLAG + ((grp * 8u + w) * 2u) * 128u + mem * 8u : 0x7fffffffu;
    o.tag = tag;
    op_all(c, o, mkseq<15>{});
    asm volatile("s_waitcnt vmcnt(0)" ::: "memory");
    u32x2 t = {(unsigned)tag, (unsigned)(tag >> 32)};
    __builtin_amdgcn_raw_buffer_store_b64(t, c.sWs, l == 0 ? o.fl + 128u : 0x7fffffffu, 0, 16);
  }
  STAMP_AT(5);
  if (w < 7u) {
    const unsigned wsrc = mem >> 1;
    const unsigned fa = WS_FLAG + ((grp * 8u + wsrc) * 2u + (w >= 5u ? 1u : 0u)) * 128u + (l & 15u) * 8u;
    #pragma nounroll
    for (unsigned spins = 0; spins < (1u << 20); ++spins) {
      u32x2 fv;
      asm volatile("buffer_load_dwordx2 %0, %1, %2, 0 offen sc1\n\ts_waitcnt vmcnt(0)" : "=v"(fv) : "v"(fa), "s"(c.sWs) : "memory");
      if (__all(fv[0] == (unsigned)tag && fv[1] == (unsigned)(tag >> 32))) break;
      __builtin_amdgcn_s_sleep(1);
    }
    const unsigned pa = WS_PART + (grp * 16u + (l >> 5)) * (7u * 8192u) + w * 8192u + wsrc * 1024u + (mem & 1u) * 512u + (l & 31u) * 16u;
    u32x4 r[8];
    #pragma unroll
    for (int i = 0; i < 8; ++i) r[i] = __builtin_amdgcn_raw_buffer_load_b128(c.sWs, pa, i * (2u * 7u * 8192u), 16);
    float a[8];
    #pragma unroll
    for (int e = 0; e < 8; ++e) a[e] = 0.f;
    #pragma unroll
    for (int i = 0; i < 8; ++i)
      #pragma unroll
      for (int e = 0; e < 4; ++e) { a[2 * e] += __uint_as_float(r[i][e] << 16); a[2 * e + 1] += __uint_as_float(r[i][e] & 0xffff0000u); }
    #pragma unroll
    for (int e = 0; e < 8; ++e) a[e] += __shfl_xor(a[e], 32);
    if (l < 32u) {
      const unsigned ls = (mem & 1u) * 32u + l, gs = ls >> 4;
      const unsigned qq = 16u * (2u * (wsrc >> 2) + (gs & 1u)) + (ls & 15u);
      const unsigned n = 64u * w + 16u * (wsrc & 3u) + 8u * (gs >> 1);
      u32x4 v = {cvtpk(a[0], a[1]), cvtpk(a[2], a[3]), cvtpk(a[4], a[5]), cvtpk(a[6], a[7])};
      *(u32x4*)(out + (size_t)(b * 64u + qq) * 1792u + br * 896u + n * 2u) = v;
    }
    if (w == 6u && mem == 0u) {
      u32x2 t = {(unsigned)tag, (unsigned)(tag >> 32)};
      __builtin_amdgcn_raw_buffer_store_b64(t, c.sWs, l == 0 ? WS_EPOCH + grp * 256u : 0x7fffffffu, 0, 16);
    }
  }
  STAMP_AT(6);
}

__global__ void amp_spin(const unsigned long long* st, int k) {
  unsigned long long mn = ~0ull, acc = 0, mx = 0;
  const unsigned b = threadIdx.x;
  unsigned long long s0 = st[b * 16], sk = st[b * 16 + (k & 7)];
  __shared__ unsigned long long a0[256], ak[256];
  a0[b] = s0; ak[b] = sk; __syncthreads();
  for (int i = 0; i < 256; ++i) { mn = a0[i] < mn ? a0[i] : mn; mx = ak[i] > mx ? ak[i] : mx; }
  for (int i = 0; i < 256; ++i) acc += ak[i] - mn;
  unsigned long long d = (k & 8) ? (mx - mn) * 256 : acc;
  if (k >= 16) { d = 0; for (int i = 0; i < 256; ++i) d += (st[i * 16 + 8 + (k & 7)] - st[i * 16 + 8]) / 20; }
  d = d * 50 / 256; if (d > 1000000) d = 1000000;
  unsigned long long t0 = __builtin_amdgcn_s_memrealtime();
  while (__builtin_amdgcn_s_memrealtime() - t0 < d) __builtin_amdgcn_s_sleep(8);
}
extern "C" void kernel_launch(void* const* d_in, const int* in_sizes, int n_in, void* d_out, int out_size, void* d_ws, size_t ws_size, hipStream_t stream) {
  (void)in_sizes; (void)n_in; (void)out_size; (void)ws_size;
  (void)hipFuncSetAttribute((const void*)gqa2_fused, hipFuncAttributeMaxDynamicSharedMemorySize, 163840);
  hipLaunchKernelGGL(gqa2_fused, dim3(256), dim3(512), 163840, stream,
                     (const char*)d_in[0], (const char*)d_in[1], (const char*)d_in[2], (const char*)d_in[3], (const char*)d_in[4],
                     (const char*)d_in[5], (const char*)d_in[6], (const char*)d_in[7], (const char*)d_in[8], (const char*)d_in[9],
                     (char*)d_out, (char*)d_ws);
  hipLaunchKernelGGL(amp_spin, dim3(1), dim3(256), 0, stream, (const unsigned long long*)((char*)d_ws + WS_STAMP), 1);
}
```

```cpp
#include <hip/hip_runtime.h>
#include <utility>

typedef __bf16   bf16x8 __attribute__((ext_vector_type(8)));
typedef __bf16   bf16x4 __attribute__((ext_vector_type(4)));
typedef __bf16   bf16x2 __attribute__((ext_vector_type(2)));
typedef float    f32x4  __attribute__((ext_vector_type(4)));
typedef float    f32x2  __attribute__((ext_vector_type(2)));
typedef unsigned u32x4  __attribute__((ext_vector_type(4)));
typedef unsigned u32x2  __attribute__((ext_vector_type(2)));
typedef unsigned long long u64;
typedef __amdgpu_buffer_rsrc_t srd_t;
#define LAS __attribute__((address_space(3)))
#define DI __device__ __forceinline__
#define PIN() __builtin_amdgcn_sched_barrier(0)
template <int... Is> using iseq = std::integer_sequence<int, Is...>;
template <int N> using mkseq = std::make_integer_sequence<int, N>;

#define STAMP 0

constexpr unsigned WS_EPOCH = 0;
constexpr unsigned WS_FLAG  = 4096;
constexpr unsigned WS_LSE   = WS_FLAG + 32768;
constexpr unsigned WS_PART  = 1u << 20;
constexpr unsigned WS_PART_BYTES = 256u * 7u * 8192u;
constexpr unsigned WS_END   = WS_PART + WS_PART_BYTES;
constexpr unsigned WS_STAMP = 32u << 20;
constexpr int NSTAMP = 7;

constexpr int PT_H = 1, PT_A = 15, PT_SM = 31, PT_B = 35, PT_C = 51, NPT = 65;
enum { IT_X = 0, IT_W = 1, IT_S = 2, IT_WO = 3, IT_TOUCH = 4, IT_LSE = 5 };
struct Item { int type, idx, pt, ops; };
struct Sched { Item it[96]; int n; };
constexpr int slotS(int n)  { constexpr int c[10] = {6, 7, 8, 9, 4, 5, 2, 3, 0, 1}; return c[n % 10]; }
constexpr int slotWO(int i) { constexpr int c[10] = {8, 9, 4, 5, 6, 7, 0, 1, 2, 3}; return c[i % 10]; }
constexpr int slotW(int t, int hh) { return 2 * (t % 3) + hh; }
constexpr int ptS[32] = {
    PT_H + 1, PT_H + 1, PT_H + 5, PT_H + 5, PT_H + 12, PT_H + 12, PT_A + 0,
    PT_A + 0, PT_A + 1, PT_A + 3, PT_A + 5, PT_A + 7, PT_A + 8, PT_A + 9, PT_A + 10, PT_A + 11,
    PT_A + 12, PT_A + 13, PT_A + 14, PT_A + 15, PT_SM + 1, PT_SM + 2, PT_SM + 3,
    PT_B + 0, PT_B + 1, PT_B + 3, PT_B + 5, PT_B + 7, PT_B + 9, PT_B + 10, PT_B + 11, PT_B + 12};
constexpr int ptWO[28] = {
    PT_B + 12, PT_B + 13, PT_B + 13, PT_B + 14, PT_B + 15, PT_C + 0,
    PT_C + 1, PT_C + 1, PT_C + 1, PT_C + 2, PT_C + 2, PT_C + 2, PT_C + 3, PT_C + 3, PT_C + 3, PT_C + 4, PT_C + 4, PT_C + 4,
    PT_C + 5, PT_C + 5, PT_C + 6, PT_C + 6, PT_C + 7, PT_C + 7, PT_C + 8, PT_C + 8, PT_C + 9, PT_C + 9};
constexpr int PT_TOUCH = PT_H + 8;
constexpr int PT_LSE_LD = PT_B + 8, PT_LSE_USE = PT_B + 12;
constexpr int ptW(int t) { return t < 3 ? 0 : PT_H + t - 2; }
constexpr bool drains(int p) { return p <= PT_A || p >= PT_B + 10; }

constexpr Sched make_sched() {
  Sched s{}; int n = 0;
  for (int p = 0; p < NPT; ++p) {
    if (p == 0) {
      s.it[n++] = Item{IT_X, 0, 0, 7};
      s.it[n++] = Item{IT_W, 0, 0, 4};
      s.it[n++] = Item{IT_W, 1, 0, 4};
      s.it[n++] = Item{IT_W, 2, 0, 4};
      continue;
    }
    for (int t = 3; t < 14; ++t) if (ptW(t) == p) s.it[n++] = Item{IT_W, t, p, 4};
    for (int i = 0; i < 32; ++i) if (ptS[i] == p) s.it[n++] = Item{IT_S, i, p, 2};
    for (int i = 0; i < 28; ++i) if (ptWO[i] == p) s.it[n++] = Item{IT_WO, i, p, 2};
    if (p == PT_TOUCH) s.it[n++] = Item{IT_TOUCH, 0, p, 1};
    if (p == PT_LSE_LD) s.it[n++] = Item{IT_LSE, 0, p, 1};
  }
  s.n = n; return s;
}
constexpr Sched SCH = make_sched();
constexpr int find_item(int type, int idx) { for (int i = 0; i < SCH.n; ++i) if (SCH.it[i].type == type && SCH.it[i].idx == idx) return i; return -1000; }
constexpr int ops_after(int L, int p) { int c = 0; for (int i = L + 1; i < SCH.n; ++i) if (SCH.it[i].pt < p) c += SCH.it[i].ops; return c; }
constexpr int imax(int a, int b) { return a > b ? a : b; }
constexpr int need_item(int p) {
  int L = -1000;
  if (p >= PT_H && p < PT_A) L = find_item(IT_W, p - PT_H);
  else if (p >= PT_A && p < PT_SM) L = find_item(IT_S, p - PT_A);
  else if (p >= PT_B && p < PT_C) L = find_item(IT_S, 16 + p - PT_B);
  else if (p >= PT_C) L = imax(find_item(IT_WO, 2 * (p - PT_C)), find_item(IT_WO, 2 * (p - PT_C) + 1));
  if (p == PT_LSE_USE) L = imax(L, find_item(IT_LSE, 0));
  return L;
}
constexpr int wait_cnt(int p) { const int c = ops_after(need_item(p), p); return c > 63 ? 63 : c; }
constexpr bool is_bar(int p) { return p >= 1 && !(p >= PT_SM && p < PT_B); }
constexpr int free_at(int use) {
  int p = use + 1; while (p < NPT && !is_bar(p)) ++p;
  if (p < NPT && (drains(p) || p != use + 1)) return p;
  ++p; while (p < NPT && !is_bar(p)) ++p;
  return p;
}
constexpr bool sched_ok() {
  for (int p = 1; p < NPT; ++p) { if (!is_bar(p)) continue; int L = need_item(p); if (L < 0 || SCH.it[L].pt >= p) return false; }
  int fr[10] = {0, 0, 0, 0, 0, 0, PT_H + 1, PT_H + 1, PT_H + 1, PT_H + 1};
  for (int i = 0; i < SCH.n; ++i) {
    const Item& I = SCH.it[i]; int sl[2] = {-1, -1}; int use = 0;
    if (I.type == IT_W) { sl[0] = slotW(I.idx, 0); sl[1] = slotW(I.idx, 1); use = PT_H + I.idx; }
    else if (I.type == IT_S) { sl[0] = slotS(I.idx); use = I.idx < 16 ? PT_A + I.idx : PT_B + I.idx - 16;
      if (sl[0] < 2 && I.pt <= PT_A) return false;
      if (sl[0] < 4 && free_at(use) > PT_B + 15) return false; }
    else if (I.type == IT_WO) { sl[0] = slotWO(I.idx); use = PT_C + I.idx / 2; if (sl[0] < 4 && I.pt <= PT_C) return false; }
    for (int k = 0; k < 2; ++k) if (sl[k] >= 0) { if (I.pt < fr[sl[k]]) return false; fr[sl[k]] = free_at(use); }
  }
  if (slotW(12, 0) != 0) return false;
  return true;
}
static_assert(sched_ok(), "schedule");
constexpr int npieces(int p) { int c = 0; for (int i = 0; i < SCH.n; ++i) if (SCH.it[i].pt == p) c += SCH.it[i].ops; return c; }
constexpr int piece_item(int p, int k) { for (int i = 0; i < SCH.n; ++i) if (SCH.it[i].pt == p) { if (k < SCH.it[i].ops) return i; k -= SCH.it[i].ops; } return -1; }
constexpr int piece_sub(int p, int k) { for (int i = 0; i < SCH.n; ++i) if (SCH.it[i].pt == p) { if (k < SCH.it[i].ops) return k; k -= SCH.it[i].ops; } return -1; }

DI unsigned cvtpk(float lo, float hi) { f32x2 v = {lo, hi}; bf16x2 b = __builtin_convertvector(v, bf16x2); return __builtin_bit_cast(unsigned, b); }
DI f32x4 mfma16(bf16x8 a, bf16x8 b, f32x4 c) { return __builtin_amdgcn_mfma_f32_16x16x32_bf16(a, b, c, 0, 0, 0); }
DI bf16x8 trfrag(unsigned a0, unsigned a1) {
  bf16x4 lo = __builtin_amdgcn_ds_read_tr16_b64_v4bf16((LAS bf16x4*)(size_t)a0);
  bf16x4 hi = __builtin_amdgcn_ds_read_tr16_b64_v4bf16((LAS bf16x4*)(size_t)a1);
  return __builtin_shufflevector(lo, hi, 0, 1, 2, 3, 4, 5, 6, 7);
}
DI unsigned opq(unsigned a) { asm volatile("" : "+v"(a)); return a; }
DI bf16x8 ldsfrag(unsigned a) { return *(const LAS bf16x8*)(size_t)a; }
template <unsigned M0, int IMM, unsigned SO, bool NT> DI void dma1(unsigned wb, unsigned v, srd_t srd) {
  unsigned t;
  if constexpr (NT) asm volatile("s_add_u32 m0, %1, %4\n\ts_mov_b32 %0, %6\n\tbuffer_load_dwordx4 %2, %3, %0 offen offset:%5 nt lds"
                                 : "=&s"(t) : "s"(wb), "v"(v), "s"(srd), "i"(M0 - IMM), "i"(IMM), "i"(SO) : "memory", "scc");
  else              asm volatile("s_add_u32 m0, %1, %4\n\ts_mov_b32 %0, %6\n\tbuffer_load_dwordx4 %2, %3, %0 offen offset:%5 lds"
                                 : "=&s"(t) : "s"(wb), "v"(v), "s"(srd), "i"(M0 - IMM), "i"(IMM), "i"(SO) : "memory", "scc");
}

struct Ctx {
  srd_t sK, sV, sWq, sWo, sX, sWs;
  unsigned ldsb, wb, xb, sw;
  unsigned vK0, vK1, vV, vWq, vWo, vX;
  unsigned vTouch, vLse, dummy;
  u32x2 lse_g;
};

template <int I, int SUB> DI void issue_piece(Ctx& c) {
  constexpr Item it = SCH.it[I];
  if constexpr (it.type == IT_X) {
    dma1<SUB * 1024u, SUB * 128, 0u, false>(c.xb, c.vX, c.sX);
  } else if constexpr (it.type == IT_W) {
    constexpr unsigned m = slotW(it.idx, SUB >> 1) * 16384u + (SUB & 1) * 1024u;
    constexpr int imm = (SUB >> 1) * 512 + (SUB & 1) * 128;
    if constexpr (SUB < 3)
      asm volatile("s_add_u32 m0, %1, %4\n\ts_nop 0\n\tbuffer_load_dwordx4 %2, %3, %0 offen offset:%5 lds"
                   :: "s"(c.sw), "s"(c.wb), "v"(c.vWq), "s"(c.sWq), "i"(m - imm), "i"(imm) : "memory", "scc");
    else
      asm volatile("s_add_u32 m0, %1, %4\n\ts_nop 0\n\tbuffer_load_dwordx4 %2, %3, %0 offen offset:%5 lds\n\t"
                   "s_add_u32 %0, %0, 0x40000\n\ts_cmp_eq_u32 %0, 0x380000\n\ts_cselect_b32 %0, 0, %0"
                   : "+s"(c.sw) : "s"(c.wb), "v"(c.vWq), "s"(c.sWq), "i"(m - imm), "i"(imm) : "memory", "scc");
  } else if constexpr (it.type == IT_S) {
    constexpr unsigned m = slotS(it.idx) * 16384u + SUB * 1024u;
    if constexpr (it.idx < 16) dma1<m, SUB * 1024, it.idx * 16384u, true>(c.wb, SUB ? c.vK1 : c.vK0, c.sK);
    else dma1<m, SUB * 128, (it.idx - 16) * 16384u, true>(c.wb, c.vV, c.sV);
  } else if constexpr (it.type == IT_WO) {
    constexpr unsigned m = slotWO(it.idx) * 16384u + SUB * 1024u;
    constexpr unsigned so = (it.idx & 3) * (128u * 896u) + (it.idx >> 2) * 128u + SUB * (8u * 896u);
    dma1<m, 0, so, false>(c.wb, c.vWo, c.sWo);
  } else if constexpr (it.type == IT_TOUCH) {
    asm volatile("buffer_load_dword %0, %1, %2, 0 offen" : "+v"(c.dummy) : "v"(c.vTouch), "s"(c.sWo) : "memory");
  } else if constexpr (it.type == IT_LSE) {
    asm volatile("buffer_load_dwordx2 %0, %1, %2, 0 offen sc1" : "=v"(c.lse_g) : "v"(c.vLse), "s"(c.sWs) : "memory");
  }
}
template <int P, int G, int g, int K> DI void issue_one(Ctx& c) { if constexpr (K * G / npieces(P) == g) issue_piece<piece_item(P, K), piece_sub(P, K)>(c); }
template <int P, int G, int g, int... Ks> DI void issue_grp_(Ctx& c, iseq<Ks...>) { (issue_one<P, G, g, Ks>(c), ...); }
template <int P, int G, int g> DI void issue_grp(Ctx& c) { issue_grp_<P, G, g>(c, mkseq<npieces(P)>{}); }

#define FINE(P, K) do {} while (0)
template <int P> DI void step_sync(Ctx& c) {
  PIN();
  if constexpr (P == PT_LSE_USE) asm volatile("s_waitcnt vmcnt(%1) lgkmcnt(0)\n\ts_barrier" : "+v"(c.lse_g) : "i"(wait_cnt(P)) : "memory");
  else if constexpr (drains(P)) asm volatile("s_waitcnt vmcnt(%0) lgkmcnt(0)\n\ts_barrier" :: "i"(wait_cnt(P)) : "memory");
  else asm volatile("s_waitcnt vmcnt(%0)\n\ts_barrier" :: "i"(wait_cnt(P)) : "memory");
  PIN();
}

#define STAMP_AT(i) do {} while (0)

struct HCtx { f32x4 acc[2][8]; bf16x8 xf[2][14]; bf16x8 fr[2][8]; unsigned tb0, tb1; };
template <int T, int g> DI void head_grp(Ctx& c, HCtx& h, unsigned b0, unsigned b1) {
  if constexpr (T >= 1) {
    h.acc[0][g] = mfma16(h.fr[(T - 1) & 1][g], h.xf[0][T - 1], h.acc[0][g]);
    h.acc[1][g] = mfma16(h.fr[(T - 1) & 1][g], h.xf[1][T - 1], h.acc[1][g]);
  }
  if constexpr (T < 14) {
    if constexpr (g < 4) {
      h.fr[T & 1][2 * g]     = trfrag(b0 + g * 512u, b0 + g * 512u + 8192u);
      h.fr[T & 1][2 * g + 1] = trfrag(b1 + g * 512u, b1 + g * 512u + 8192u);
    }
    issue_grp<PT_H + T, 8, g>(c);
  }
  PIN();
}
template <int T, int... gs> DI void head_step(Ctx& c, HCtx& h, iseq<gs...>) {
  unsigned b0 = 0, b1 = 0;
  if constexpr (T < 14) {
    if constexpr (T > 0) step_sync<PT_H + T>(c);
    b0 = opq(h.tb0 + slotW(T, 0) * 16384u); b1 = opq(h.tb1 + slotW(T, 0) * 16384u);
  }
  (head_grp<T, gs>(c, h, b0, b1), ...);
}
template <int... Ts> DI void head_all(Ctx& c, HCtx& h, iseq<Ts...>) { (head_step<Ts>(c, h, mkseq<8>{}), ...); }

struct QCtx { f32x4 S[32]; bf16x8 qf[8]; bf16x8 fr[8]; unsigned kb[4], kk[4]; };
template <int F> DI void qk_frag(Ctx& c, QCtx& q) {
  constexpr int N = F >> 4, i = F & 15;
  if constexpr (F < 256 && i == 0) {
    if constexpr (N > 0) step_sync<PT_A + N>(c);
    #pragma unroll
    for (int u = 0; u < 4; ++u) q.kk[u] = opq(q.kb[u] + slotS(N) * 16384u);
  }
  if constexpr (F >= 8) {
    constexpr int M = F - 8, mt = 2 * (M >> 4) + (M & 1), mu = (M & 15) >> 1;
    if constexpr (mu == 0) q.S[mt] = mfma16(q.fr[M & 7], q.qf[mu], f32x4{0.f, 0.f, 0.f, 0.f});
    else q.S[mt] = mfma16(q.fr[M & 7], q.qf[mu], q.S[mt]);
  }
  if constexpr (F < 256) {
    constexpr int u = i >> 1, a = i & 1;
    q.fr[F & 7] = ldsfrag(q.kk[u & 3] + (u >> 2) * 256u + a * 8192u);
    if constexpr (npieces(PT_A + N) > 0 && (i & 1) == 0 && i < 8) issue_grp<PT_A + N, 4, (i >> 1)>(c);
  }
  PIN();
}
template <int... Fs> DI void qk_all(Ctx& c, QCtx& q, iseq<Fs...>) { (qk_frag<Fs>(c, q), ...); }

template <int Q> DI void sm_quarter(Ctx& c, f32x4 (&S)[32], bf16x8 (&pf)[16], float cs, float nm, float& l0, float& l1) {
  if constexpr (npieces(PT_SM + Q) > 0) issue_grp<PT_SM + Q, 1, 0>(c);
  #pragma unroll
  for (int t = 4 * Q; t < 4 * Q + 4; ++t) {
    float p[8];
    #pragma unroll
    for (int e = 0; e < 8; ++e) p[e] = __builtin_amdgcn_exp2f(__builtin_fmaf(S[2 * t + (e >> 2)][e & 3], cs, nm));
    l0 += (p[0] + p[1]) + (p[2] + p[3]); l1 += (p[4] + p[5]) + (p[6] + p[7]);
    u32x4 wv = {cvtpk(p[0], p[1]), cvtpk(p[2], p[3]), cvtpk(p[4], p[5]), cvtpk(p[6], p[7])};
    pf[t] = __builtin_bit_cast(bf16x8, wv);
  }
  PIN();
}

struct PCtx { f32x4 O[16]; bf16x8 pf[16]; bf16x8 fr[8]; unsigned tb0, tb1, b0, b1; };
template <int F> DI void pv_frag(Ctx& c, PCtx& p) {
  constexpr int N = F >> 4, db = F & 15;
  if constexpr (F < 256 && db == 0) {
    step_sync<PT_B + N>(c);
    p.b0 = opq(p.tb0 + slotS(16 + N) * 16384u); p.b1 = opq(p.tb1 + slotS(16 + N) * 16384u);
  }
  if constexpr (F >= 8) {
    constexpr int M = F - 8;
    if constexpr (M < 16) p.O[M & 15] = mfma16(p.fr[M & 7], p.pf[M >> 4], f32x4{0.f, 0.f, 0.f, 0.f});
    else p.O[M & 15] = mfma16(p.fr[M & 7], p.pf[M >> 4], p.O[M & 15]);
  }
  if constexpr (F < 256) {
    const unsigned a = ((db & 1) ? p.b1 : p.b0) + (db >> 1) * 512u;
    p.fr[F & 7] = trfrag(a, a + 8192u);
    if constexpr (npieces(PT_B + N) > 0 && (db & 1) == 0 && db < 8) issue_grp<PT_B + N, 4, (db >> 1)>(c);
  }
  PIN();
}
template <int... Fs> DI void pv_all(Ctx& c, PCtx& p, iseq<Fs...>) { (pv_frag<Fs>(c, p), ...); }

struct OCtx { f32x4 a0, a1; bf16x8 of[2][16]; bf16x8 fr[2][8]; unsigned wb, ob, pst, fl; u64 tag; };
template <int I, int g> DI void op_grp(Ctx& c, OCtx& o, unsigned w0, unsigned w1) {
  if constexpr (I >= 1) {
    constexpr int J = I - 1, U = 8 * (J & 1) + g;
    if constexpr (U == 0) { o.a0 = mfma16(o.fr[J & 1][g], o.of[0][U], f32x4{0.f, 0.f, 0.f, 0.f}); o.a1 = mfma16(o.fr[J & 1][g], o.of[1][U], f32x4{0.f, 0.f, 0.f, 0.f}); }
    else { o.a0 = mfma16(o.fr[J & 1][g], o.of[0][U], o.a0); o.a1 = mfma16(o.fr[J & 1][g], o.of[1][U], o.a1); }
  }
  if constexpr (I < 14) {
    if constexpr (g < 4) {
      const unsigned a = (g < 2 ? w0 : w1) + (g & 1) * 8192u;
      o.fr[I & 1][2 * g]     = trfrag(a, a + 2048u);
      o.fr[I & 1][2 * g + 1] = trfrag(a + 4096u, a + 6144u);
    }
    if constexpr (I == 0) {
      #pragma unroll
      for (int U = 2 * g; U < 2 * g + 2; ++U)
        #pragma unroll
        for (int z = 0; z < 2; ++z) o.of[z][U] = ldsfrag(o.ob + z * 8192u + (U >> 3) * 32768u + (U & 7) * 1024u);
    }
    if constexpr (npieces(PT_C + I) > 0) issue_grp<PT_C + I, 8, g>(c);
  }
  PIN();
}
template <int I, int... gs> DI void op_step(Ctx& c, OCtx& o, iseq<gs...>) {
  unsigned w0 = 0, w1 = 0;
  if constexpr (I < 14) {
    step_sync<PT_C + I>(c);
    w0 = opq(o.wb + slotWO(2 * I) * 16384u); w1 = opq(o.wb + slotWO(2 * I + 1) * 16384u);
  }
  (op_grp<I, gs>(c, o, w0, w1), ...);
  if constexpr (I >= 2 && (I & 1) == 0) {
    constexpr int ch = (I - 2) >> 1;
    unsigned x0 = cvtpk(o.a0[0], o.a0[1]), x1 = cvtpk(o.a0[2], o.a0[3]), y0 = cvtpk(o.a1[0], o.a1[1]), y1 = cvtpk(o.a1[2], o.a1[3]);
    auto r0 = __builtin_amdgcn_permlane16_swap(x0, y0, false, false);
    auto r1 = __builtin_amdgcn_permlane16_swap(x1, y1, false, false);
    u32x4 v = {r0[0], r1[0], r0[1], r1[1]};
    if constexpr (ch == 5) {
      asm volatile("s_waitcnt vmcnt(0)" ::: "memory");
      u32x2 t = {(unsigned)o.tag, (unsigned)(o.tag >> 32)};
      __builtin_amdgcn_raw_buffer_store_b64(t, c.sWs, o.fl, 0, 16);
    }
    __builtin_amdgcn_raw_buffer_store_b128(v, c.sWs, o.pst, ch * 8192u, 16);
  }
}
template <int... Is> DI void op_all(Ctx& c, OCtx& o, iseq<Is...>) { (op_step<Is>(c, o, mkseq<8>{}), ...); }

__global__ __launch_bounds__(512, 2)
void gqa2_fused(const char* __restrict__ xA, const char* __restrict__ kA, const char* __restrict__ vA, const char* __restrict__ wqA, const char* __restrict__ woA,
                const char* __restrict__ xB, const char* __restrict__ kB, const char* __restrict__ vB, const char* __restrict__ wqB, const char* __restrict__ woB,
                char* __restrict__ out, char* __restrict__ ws) {
  extern __shared__ __attribute__((aligned(16))) char lds[];
  u64 g_now; asm volatile("s_memrealtime %0" : "=s"(g_now) :: "memory");
  __builtin_amdgcn_sched_barrier(0);
  const unsigned tid = threadIdx.x, l = tid & 63u;
  const unsigned w = __builtin_amdgcn_readfirstlane(tid >> 6);
  const unsigned bid = blockIdx.x, kvh = bid & 7u, j = bid >> 3, s = j & 1u, b = (j >> 1) & 7u, br = j >> 4;
  const unsigned grp = j >> 1, mem = kvh * 2u + s, wg = grp * 16u + mem;
  const unsigned q16 = l & 15u, g = l >> 4, hh = w >> 2;
  STAMP_AT(0);
  Ctx c;
  const unsigned rot = (j & 15u) >= 14u ? (j & 15u) - 9u : (j & 15u);
  c.ldsb = (unsigned)(size_t)lds; c.wb = c.ldsb + w * 2048u; c.sw = rot << 18; c.xb = c.ldsb + 6u * 16384u + w * 7168u;
  c.sX  = __builtin_amdgcn_make_buffer_rsrc((void*)((br ? xB : xA) + b * (64u * 896u)), 0, 64 * 896, 0x00020000);
  c.sWq = __builtin_amdgcn_make_buffer_rsrc((void*)((br ? wqB : wqA) + kvh * 1024u), 0, 448 * 8192 - kvh * 1024u, 0x00020000);
  const unsigned hi = l >> 5, pos = l & 31u, row = (l >> 2) & 7u, cc = (l & 3u) ^ ((row >> 2) << 1);
  const unsigned rg = w >> 1, col = hi * 64u + cc * 16u;
  c.vX  = (8u * w + (l >> 3)) * 896u + (((l & 7u) ^ (((w & 1u) << 2) | (l >> 4))) * 16u);
  c.vWq = (16u * (rg & 1u) + 4u * (rg >> 1) + 8u * (row >> 2) + (row & 3u)) * 8192u + (w & 1u) * 256u + col;
  issue_grp<0, 1, 0>(c);
  PIN();
  const size_t kvoff = ((size_t)(b * 8u + kvh) * 1024u + s * 512u) * 512u;
  c.sK  = __builtin_amdgcn_make_buffer_rsrc((void*)((br ? kB : kA) + kvoff), 0, 262144, 0x00020000);
  c.sV  = __builtin_amdgcn_make_buffer_rsrc((void*)((br ? vB : vA) + kvoff), 0, 262144, 0x00020000);
  c.sWo = __builtin_amdgcn_make_buffer_rsrc((void*)((br ? woB : woA) + kvh * (512u * 896u)), 0, 512 * 896, 0x00020000);
  c.sWs = __builtin_amdgcn_make_buffer_rsrc((void*)ws, 0, WS_END, 0x00020000);
  {
    const unsigned kin = hi * 512u + ((pos ^ hi) * 16u);
    c.vK0 = w * 2048u + (kin ^ (((2u * w) & 7u) * 32u));
    c.vK1 = w * 2048u + (kin ^ (((2u * w + 1u) & 7u) * 32u));
    c.vV  = (rg * 8u + row) * 512u + (w & 1u) * 256u + col;
    c.vWo = (16u * w + row) * 896u + col;
    c.vTouch = tid < 224u ? ((j & 15u) * 224u + tid) * 128u : 0x7fffffffu;
    c.vLse = WS_LSE + (wg ^ 1u) * 1024u + w * 128u + q16 * 8u;
    c.dummy = 0;
  }
  u64 epoch;
  asm volatile("s_load_dwordx2 %0, %1, %2\n\ts_waitcnt lgkmcnt(0)" : "=s"(epoch) : "s"(ws), "s"(grp * 256u) : "memory");
  const u64 tag = epoch + 0x9E3779B97F4A7C15ull;

  const unsigned tp = l & 3u, tr = (l >> 2) & 3u;
  const unsigned tin = (4u * (g & 1u) + tr) * 64u + tp * 8u;
  const unsigned tb0 = c.ldsb + (g >> 1) * 4096u + tin + (g & 1u) * 32u;
  const unsigned tb1 = c.ldsb + (g >> 1) * 4096u + tin + ((g & 1u) ^ 1u) * 32u;

  QCtx q;
  {
    HCtx h;
    const unsigned bb = w & 1u;
    h.tb0 = tb0 + hh * 16384u + bb * 2048u; h.tb1 = tb1 + hh * 16384u + bb * 2048u;
    #pragma unroll
    for (int i = 0; i < 8; ++i) { h.acc[0][i] = f32x4{0.f, 0.f, 0.f, 0.f}; h.acc[1][i] = f32x4{0.f, 0.f, 0.f, 0.f}; }
    step_sync<PT_H>(c);
    {
      const unsigned sw = q16 >> 1;
      unsigned t = rot;
      #pragma unroll
      for (int i = 0; i < 14; ++i) {
        const unsigned in = (t >> 1) * 1024u + (q16 & 7u) * 128u + (((((t & 1u) << 2) | g) ^ sw) * 16u);
        #pragma unroll
        for (int z = 0; z < 2; ++z) {
          const unsigned qz = (w & 3u) ^ z;
          h.xf[z][i] = ldsfrag(c.ldsb + 6u * 16384u + (2u * qz + (q16 >> 3)) * 7168u + in);
        }
        t = t == 13u ? 0u : t + 1u;
      }
    }
    PIN();
    head_all(c, h, mkseq<15>{});
    u32x4 own[4];
    #pragma unroll
    for (int z = 0; z < 2; ++z)
      #pragma unroll
      for (int u = 0; u < 4; ++u) {
        unsigned a0 = cvtpk(h.acc[z][2 * u][0], h.acc[z][2 * u][1]), a1 = cvtpk(h.acc[z][2 * u][2], h.acc[z][2 * u][3]);
        unsigned b0 = cvtpk(h.acc[z][2 * u + 1][0], h.acc[z][2 * u + 1][1]), b1 = cvtpk(h.acc[z][2 * u + 1][2], h.acc[z][2 * u + 1][3]);
        auto r0 = __builtin_amdgcn_permlane16_swap(a0, b0, false, false);
        auto r1 = __builtin_amdgcn_permlane16_swap(a1, b1, false, false);
        u32x4 v = {r0[0], r1[0], r0[1], r1[1]};
        if (z == 0) own[u] = v;
        else *(LAS u32x4*)(size_t)(c.ldsb + w * 4096u + u * 1024u + l * 16u) = v;
      }
    STAMP_AT(1);
    step_sync<PT_A>(c);
    #pragma unroll
    for (int u = 0; u < 4; ++u) {
      const u32x4 rv = *(const LAS u32x4*)(size_t)(c.ldsb + (w ^ 1u) * 4096u + u * 1024u + l * 16u);
      q.qf[u]     = __builtin_bit_cast(bf16x8, bb ? rv : own[u]);
      q.qf[4 + u] = __builtin_bit_cast(bf16x8, bb ? own[u] : rv);
    }
  }
  PCtx p;
  float lsum, lse;
  {
    const unsigned cm = ((g & 1u) << 1) | (g >> 1);
    #pragma unroll
    for (int u = 0; u < 4; ++u) q.kb[u] = c.ldsb + q16 * 512u + (((4u * u + cm) ^ q16) * 16u);
    qk_all(c, q, mkseq<264>{});
    asm volatile("" : "+v"(c.dummy));
    STAMP_AT(2);
    float m0 = q.S[0][0], m1 = q.S[0][1];
    #pragma unroll
    for (int i = 0; i < 32; ++i) { m0 = fmaxf(fmaxf(m0, q.S[i][0]), q.S[i][1]); m1 = fmaxf(fmaxf(m1, q.S[i][2]), q.S[i][3]); }
    float m = fmaxf(m0, m1);
    m = fmaxf(m, __shfl_xor(m, 16)); m = fmaxf(m, __shfl_xor(m, 32));
    const float cs = 0.0625f * 1.4426950408889634f;
    const float nm = -m * cs;
    float l0 = 0.f, l1 = 0.f;
    sm_quarter<0>(c, q.S, p.pf, cs, nm, l0, l1); sm_quarter<1>(c, q.S, p.pf, cs, nm, l0, l1);
    sm_quarter<2>(c, q.S, p.pf, cs, nm, l0, l1); sm_quarter<3>(c, q.S, p.pf, cs, nm, l0, l1);
    lsum = l0 + l1;
    lsum += __shfl_xor(lsum, 16); lsum += __shfl_xor(lsum, 32);
    lse = __builtin_amdgcn_logf(lsum) - nm;
    u32x2 gr = {__float_as_uint(lse), (unsigned)tag};
    __builtin_amdgcn_raw_buffer_store_b64(gr, c.sWs, WS_LSE + wg * 1024u + w * 128u + q16 * 8u, 0, 16);
  }
  STAMP_AT(3);
  OCtx o;
  {
    p.tb0 = tb0; p.tb1 = tb1;
    pv_all(c, p, mkseq<264>{});
    STAMP_AT(4);
    if (__builtin_expect(!__all(c.lse_g[1] == (unsigned)tag), 0)) {
      #pragma nounroll
      for (unsigned spins = 0; spins < (1u << 20); ++spins) {
        asm volatile("buffer_load_dwordx2 %0, %1, %2, 0 offen sc1\n\ts_waitcnt vmcnt(0)" : "=v"(c.lse_g) : "v"(c.vLse), "s"(c.sWs) : "memory");
        if (__all(c.lse_g[1] == (unsigned)tag)) break;
        __builtin_amdgcn_s_sleep(2);
      }
    }
    const float f = __builtin_amdgcn_rcpf(lsum * (1.f + __builtin_amdgcn_exp2f(__uint_as_float(c.lse_g[0]) - lse)));
    const unsigned ow = c.ldsb + w * 8192u + l * 16u;
    #pragma unroll
    for (int u = 0; u < 8; ++u) {
      u32x4 v = {cvtpk(p.O[2 * u][0] * f, p.O[2 * u][1] * f), cvtpk(p.O[2 * u][2] * f, p.O[2 * u][3] * f),
                 cvtpk(p.O[2 * u + 1][0] * f, p.O[2 * u + 1][1] * f), cvtpk(p.O[2 * u + 1][2] * f, p.O[2 * u + 1][3] * f)};
      *(LAS u32x4*)(size_t)(ow + u * 1024u) = v;
    }
  }
  {
    const unsigned nb = w & 3u;
    o.wb = c.ldsb + (g >> 1) * 1024u + (nb >> 1) * 512u + tin + (((nb & 1u) ^ (g & 1u)) * 32u);
    o.ob = c.ldsb + (w >> 2) * 16384u + l * 16u;
    o.pst = WS_PART + wg * (7u * 8192u) + w * 1024u + l * 16u;
    o.fl = l == 0 ? WS_FLAG + ((grp * 8u + w) * 2u) * 128u + mem * 8u : 0x7fffffffu;
    o.tag = tag;
    op_all(c, o, mkseq<15>{});
    asm volatile("s_waitcnt vmcnt(0)" ::: "memory");
    u32x2 t = {(unsigned)tag, (unsigned)(tag >> 32)};
    __builtin_amdgcn_raw_buffer_store_b64(t, c.sWs, l == 0 ? o.fl + 128u : 0x7fffffffu, 0, 16);
  }
  STAMP_AT(5);
  if (w < 7u) {
    const unsigned wsrc = mem >> 1;
    const unsigned fa = WS_FLAG + ((grp * 8u + wsrc) * 2u + (w >= 5u ? 1u : 0u)) * 128u + (l & 15u) * 8u;
    #pragma nounroll
    for (unsigned spins = 0; spins < (1u << 20); ++spins) {
      u32x2 fv;
      asm volatile("buffer_load_dwordx2 %0, %1, %2, 0 offen sc1\n\ts_waitcnt vmcnt(0)" : "=v"(fv) : "v"(fa), "s"(c.sWs) : "memory");
      if (__all(fv[0] == (unsigned)tag && fv[1] == (unsigned)(tag >> 32))) break;
      __builtin_amdgcn_s_sleep(1);
    }
    const unsigned pa = WS_PART + (grp * 16u + (l >> 5)) * (7u * 8192u) + w * 8192u + wsrc * 1024u + (mem & 1u) * 512u + (l & 31u) * 16u;
    u32x4 r[8];
    #pragma unroll
    for (int i = 0; i < 8; ++i) r[i] = __builtin_amdgcn_raw_buffer_load_b128(c.sWs, pa, i * (2u * 7u * 8192u), 16);
    float a[8];
    #pragma unroll
    for (int e = 0; e < 8; ++e) a[e] = 0.f;
    #pragma unroll
    for (int i = 0; i < 8; ++i)
      #pragma unroll
      for (int e = 0; e < 4; ++e) { a[2 * e] += __uint_as_float(r[i][e] << 16); a[2 * e + 1] += __uint_as_float(r[i][e] & 0xffff0000u); }
    #pragma unroll
    for (int e = 0; e < 8; ++e) a[e] += __shfl_xor(a[e], 32);
    if (l < 32u) {
      const unsigned ls = (mem & 1u) * 32u + l, gs = ls >> 4;
      const unsigned qq = 16u * (2u * (wsrc >> 2) + (gs & 1u)) + (ls & 15u);
      const unsigned n = 64u * w + 16u * (wsrc & 3u) + 8u * (gs >> 1);
      u32x4 v = {cvtpk(a[0], a[1]), cvtpk(a[2], a[3]), cvtpk(a[4], a[5]), cvtpk(a[6], a[7])};
      *(u32x4*)(out + (size_t)(b * 64u + qq) * 1792u + br * 896u + n * 2u) = v;
    }
    if (w == 6u && mem == 0u) {
      u32x2 t = {(unsigned)tag, (unsigned)(tag >> 32)};
      __builtin_amdgcn_raw_buffer_store_b64(t, c.sWs, l == 0 ? WS_EPOCH + grp * 256u : 0x7fffffffu, 0, 16);
    }
  }
  while (__builtin_amdgcn_s_memrealtime() - g_now < 8000) __builtin_amdgcn_s_sleep(2);
}

__global__ void tiny_k() {}
__global__ void spin_small() { u64 t = __builtin_amdgcn_s_memrealtime(); while (__builtin_amdgcn_s_memrealtime() - t < 1000) __builtin_amdgcn_s_sleep(2); }
__global__ __launch_bounds__(512, 2) void spin_big() { u64 t = __builtin_amdgcn_s_memrealtime(); asm volatile("v_mov_b32 v250, 0" ::: "v250"); while (__builtin_amdgcn_s_memrealtime() - t < 1000) __builtin_amdgcn_s_sleep(2); }
extern "C" void kernel_launch(void* const* d_in, const int* in_sizes, int n_in, void* d_out, int out_size, void* d_ws, size_t ws_size, hipStream_t stream) {
  (void)in_sizes; (void)n_in; (void)out_size; (void)ws_size;
  (void)hipFuncSetAttribute((const void*)gqa2_fused, hipFuncAttributeMaxDynamicSharedMemorySize, 163840);
  hipLaunchKernelGGL(gqa2_fused, dim3(256), dim3(512), 163840, stream,
                     (const char*)d_in[0], (const char*)d_in[1], (const char*)d_in[2], (const char*)d_in[3], (const char*)d_in[4],
                     (const char*)d_in[5], (const char*)d_in[6], (const char*)d_in[7], (const char*)d_in[8], (const char*)d_in[9],
                     (char*)d_out, (char*)d_ws);
}
```

```cpp
#include <hip/hip_runtime.h>
#include <type_traits>

typedef __bf16   bf16x8 __attribute__((ext_vector_type(8)));
typedef __bf16   bf16x4 __attribute__((ext_vector_type(4)));
typedef __bf16   bf16x2 __attribute__((ext_vector_type(2)));
typedef float    f32x4  __attribute__((ext_vector_type(4)));
typedef float    f32x16 __attribute__((ext_vector_type(16)));
typedef float    f32x2  __attribute__((ext_vector_type(2)));
typedef unsigned u32x4  __attribute__((ext_vector_type(4)));
typedef unsigned u32x2  __attribute__((ext_vector_type(2)));
typedef unsigned long long u64;
typedef __amdgpu_buffer_rsrc_t srd_t;
#define LAS __attribute__((address_space(3)))
#define DI __device__ __forceinline__
#define PIN() __builtin_amdgcn_sched_barrier(0)

#define STAMP 0
#define SWP 0
#define MIXPER 1
#define MIXN 1
#define LOCK_NT 1
#define CODEPF 1
#define ROTK 0
#define WOPF 0
#define PRETOUCH 0
#define NOWO 0
#define NOST 0
#define LAUX 16
#define COFF 1
#define DEEP 0
#define DBH 8
#define DB 8

constexpr unsigned WS_EPOCH = 0;
#define FS 8
constexpr unsigned WS_FLAG  = 4096;
constexpr unsigned WS_PART  = 1u << 20;
constexpr unsigned WS_PART_BYTES = 256u * 7u * 8192u;
constexpr unsigned WS_END   = WS_PART + WS_PART_BYTES;
constexpr unsigned WS_STAMP = 32u << 20;
constexpr int NSTAMP = 7, NBAR = 51, LW0 = 0;
constexpr unsigned L_XST = 163840u - 57344u;
constexpr unsigned L_EXT = 131072u;
constexpr unsigned L_AL  = 4096u;
constexpr float THR = 8.0f;
constexpr unsigned wo_slot(unsigned i) { return i < 8u ? (i + 2u) & 7u : i < 10u ? i : i - 8u; }

struct Args { const char* in[10]; char* out; char* ws; };

DI unsigned cvtpk(float lo, float hi) { f32x2 v = {lo, hi}; bf16x2 b = __builtin_convertvector(v, bf16x2); return __builtin_bit_cast(unsigned, b); }
DI f32x4 mfma16(bf16x8 a, bf16x8 b, f32x4 c) { return __builtin_amdgcn_mfma_f32_16x16x32_bf16(a, b, c, 0, 0, 0); }
DI f32x16 mfma32(bf16x8 a, bf16x8 b, f32x16 c) { return __builtin_amdgcn_mfma_f32_32x32x16_bf16(a, b, c, 0, 0, 0); }
DI bf16x8 trfrag(unsigned a0, unsigned a1) {
  bf16x4 lo = __builtin_amdgcn_ds_read_tr16_b64_v4bf16((LAS bf16x4*)(size_t)a0);
  bf16x4 hi = __builtin_amdgcn_ds_read_tr16_b64_v4bf16((LAS bf16x4*)(size_t)a1);
  return __builtin_shufflevector(lo, hi, 0, 1, 2, 3, 4, 5, 6, 7);
}
DI unsigned opq(unsigned a) { asm volatile("" : "+v"(a)); return a; }
DI bf16x8 ldsfrag(unsigned a) { return *(const LAS bf16x8*)(size_t)a; }
DI float ldsf(unsigned a) { return *(const LAS float*)(size_t)a; }
DI void cbar() { PIN(); asm volatile("s_waitcnt lgkmcnt(0)\n\ts_barrier" ::: "memory"); PIN(); }
DI void lbar() { asm volatile("s_barrier" ::: "memory"); }
template <int N> DI void vmwait() { asm volatile("s_waitcnt vmcnt(%0)" :: "i"(N) : "memory"); }

template <int H, bool NT> DI void dma_cols(unsigned m, unsigned v, srd_t srd, unsigned so) {
  if constexpr (NT)
    asm volatile("s_add_u32 m0, %0, %4\n\ts_nop 0\n\tbuffer_load_dwordx4 %1, %2, %3 offen offset:%5 nt lds\n\t"
                 "s_add_u32 m0, %0, %6\n\ts_nop 0\n\tbuffer_load_dwordx4 %1, %2, %3 offen offset:%7 nt lds"
                 :: "s"(m), "v"(v), "s"(srd), "s"(so), "i"(H * 1792), "i"(H * 256), "i"(H * 1792 + 896), "i"(H * 256 + 128) : "memory", "scc");
  else
    asm volatile("s_add_u32 m0, %0, %4\n\ts_nop 0\n\tbuffer_load_dwordx4 %1, %2, %3 offen offset:%5 lds\n\t"
                 "s_add_u32 m0, %0, %6\n\ts_nop 0\n\tbuffer_load_dwordx4 %1, %2, %3 offen offset:%7 lds"
                 :: "s"(m), "v"(v), "s"(srd), "s"(so), "i"(H * 1792), "i"(H * 256), "i"(H * 1792 + 896), "i"(H * 256 + 128) : "memory", "scc");
}
template <int H, bool NT> DI void dma_k(unsigned m, const unsigned (&v)[4], srd_t srd, unsigned so) {
  if constexpr (NT)
    asm volatile("s_mov_b32 m0, %0\n\ts_nop 0\n\tbuffer_load_dwordx4 %1, %3, %4 offen offset:%5 nt lds\n\tbuffer_load_dwordx4 %2, %3, %4 offen offset:%6 nt lds"
                 :: "s"(m), "v"(v[2 * H]), "v"(v[2 * H + 1]), "s"(srd), "s"(so), "i"(H * 2048), "i"(H * 2048 + 1024) : "memory");
  else
    asm volatile("s_mov_b32 m0, %0\n\ts_nop 0\n\tbuffer_load_dwordx4 %1, %3, %4 offen offset:%5 lds\n\tbuffer_load_dwordx4 %2, %3, %4 offen offset:%6 lds"
                 :: "s"(m), "v"(v[2 * H]), "v"(v[2 * H + 1]), "s"(srd), "s"(so), "i"(H * 2048), "i"(H * 2048 + 1024) : "memory");
}
template <int H> DI void dma_wo(unsigned m, unsigned v, srd_t srd, unsigned so) {
  unsigned t;
  asm volatile("s_add_u32 m0, %1, %5\n\ts_add_u32 %0, %4, %6\n\tbuffer_load_dwordx4 %2, %3, %0 offen lds\n\t"
               "s_add_u32 m0, %1, %7\n\ts_add_u32 %0, %4, %8\n\tbuffer_load_dwordx4 %2, %3, %0 offen lds"
               : "=&s"(t) : "s"(m), "v"(v), "s"(srd), "s"(so), "i"(H * 2048), "i"(H * 14336), "i"(H * 2048 + 1024), "i"(H * 14336 + 7168) : "memory", "scc");
}
DI void dma_one(unsigned m, unsigned v, srd_t srd, unsigned so) {
  asm volatile("s_mov_b32 m0, %0\n\ts_nop 0\n\tbuffer_load_dwordx4 %1, %2, %3 offen lds" :: "s"(m), "v"(v), "s"(srd), "s"(so) : "memory");
}

#define STAMP_AT(i) do {} while (0)
#define CST(i) do {} while (0)

#define PSLEEP 1
DI void reduce_chunk(srd_t sWs, char* out, unsigned grp, unsigned mem, unsigned ch, unsigned l, unsigned b, unsigned br, u64 tag) {
  const unsigned fa = WS_FLAG + ((grp * 4u + (mem >> 2)) * 16u + (l & 15u)) * FS;
  const unsigned pa = WS_PART + (grp * 16u + (l >> 5)) * (7u * 8192u) + ch * 8192u + mem * 512u + (l & 31u) * 16u;
  const unsigned wv = mem >> 2, q = 32u * (wv >> 1) + (l & 31u), n = 64u * ch + 32u * (wv & 1u) + 16u * (mem & 1u) + 8u * ((mem >> 1) & 1u) + 4u * (l >> 5);
  char* const po = out + (size_t)(b * 64u + q) * 1792u + br * 896u + n * 2u;
  asm volatile("" :: "v"(po), "v"(pa));
  #pragma nounroll
  for (unsigned spins = 0; spins < (1u << 20); ++spins) {
    u32x2 fv;
    asm volatile("buffer_load_dwordx2 %0, %1, %2, 0 offen sc1\n\ts_waitcnt vmcnt(0)" : "=v"(fv) : "v"(fa), "s"(sWs) : "memory");
    if (__all(fv[0] == (unsigned)tag && fv[1] == (unsigned)(tag >> 32))) break;
    if (PSLEEP) __builtin_amdgcn_s_sleep(PSLEEP);
  }
  u32x4 r[8];
  #pragma unroll
  for (int i = 0; i < 8; ++i) r[i] = __builtin_amdgcn_raw_buffer_load_b128(sWs, pa, i * (2u * 7u * 8192u), LAUX);
  float a[8];
  #pragma unroll
  for (int i = 0; i < 8; ++i) {
    #pragma unroll
    for (int e = 0; e < 4; ++e) {
      const float lo = __uint_as_float(r[i][e] << 16), hi = __uint_as_float(r[i][e] & 0xffff0000u);
      a[2 * e] = i ? a[2 * e] + lo : lo; a[2 * e + 1] = i ? a[2 * e + 1] + hi : hi;
    }
    PIN();
  }
  float o[4];
  #pragma unroll
  for (int e = 0; e < 4; ++e) {
    auto s = __builtin_amdgcn_permlane32_swap(__float_as_uint(a[e]), __float_as_uint(a[4 + e]), false, false);
    o[e] = __uint_as_float(s[0]) + __uint_as_float(s[1]);
  }
  *(u32x2*)po = u32x2{cvtpk(o[0], o[1]), cvtpk(o[2], o[3])};
}

__global__ __launch_bounds__(512, 2)
void gqa2_fused(Args args) {
  extern __shared__ __attribute__((aligned(16))) char lds[];
  u64 pc0; asm volatile("s_getpc_b64 %0\n.Lgqa_start:" : "=s"(pc0));
  const unsigned bid = blockIdx.x, br = (bid >> 2) & 1u, j = bid >> 3, hs = j & 1u, b = j >> 2;
  const char* const px = args.in[br * 5u], * const pk = args.in[br * 5u + 1u], * const pv = args.in[br * 5u + 2u], * const pwq = args.in[br * 5u + 3u], * const pwo = args.in[br * 5u + 4u];
  char* const out = args.out; char* const ws = args.ws;
  asm volatile("" :: "s"(px), "s"(pk), "s"(pv), "s"(pwq), "s"(pwo), "s"(out), "s"(ws));
  const unsigned kvh = (bid & 3u) * 2u + ((j >> 1) & 1u), grp = br * 8u + b, mem = kvh * 2u + hs;
  const unsigned tid = threadIdx.x, l = tid & 63u;
  const unsigned w8 = __builtin_amdgcn_readfirstlane(tid >> 6), w = w8 & 3u;
  const unsigned rot = b == 7u ? 3u : b;
  const unsigned ldsb = (unsigned)(size_t)lds;
  STAMP_AT(0);
  const srd_t sWs = __builtin_amdgcn_make_buffer_rsrc((void*)ws, 0, WS_END, 0x00020000);

  u64 tag;
  if (__builtin_expect(w8 < 4u, 1)) {
    const srd_t sWq = __builtin_amdgcn_make_buffer_rsrc((void*)(pwq + mem * 512u), 0, 448 * 8192 - mem * 512u, 0x00020000);
    const unsigned hi = l >> 5, row = (l >> 2) & 7u, col = hi * 64u + (l & 3u) * 16u;
    const unsigned wb = ldsb + w * 4096u;
    const unsigned vWq = (8u * w + row) * 8192u + col;
    unsigned sw = rot << 19;
    auto WA = [&](unsigned slot) { dma_cols<0, false>(wb + slot * 16384u, vWq, sWq, sw); };
    auto WB = [&](unsigned slot) { dma_cols<1, false>(wb + slot * 16384u, vWq, sWq, sw); sw += 0x40000u; sw = sw == 0x380000u ? 0u : sw; };
    WA(0); WB(0); WA(1); WB(1);
    PIN();
    u64 epoch;
    asm volatile("s_load_dwordx2 %0, %1, %2" : "=s"(epoch) : "s"(ws), "s"(grp * 256u) : "memory");
    {
      const srd_t sX = __builtin_amdgcn_make_buffer_rsrc((void*)(px + b * (64u * 896u)), 0, 64 * 896, 0x00020000);
      const unsigned xr = l >> 3;
      const unsigned v0 = xr * 896u + (((l & 7u) ^ (xr >> 1)) * 16u);
      unsigned pc = (j & 3u) * 14u + w;
      #pragma nounroll
      for (int i = 0; i < 14; ++i) {
        const unsigned rg = (pc * 37u) >> 8;
        dma_one(ldsb + L_XST + pc * 1024u, v0 ^ ((rg & 1u) << 6), sX, rg * (7168u - 896u) + pc * 128u);
        pc += 4u; pc = pc >= 56u ? pc - 56u : pc;
      }
    }
    WA(2); WB(2); WA(3); WB(3);
    const size_t kvoff = (size_t)(b * 8u + kvh) * (1024u * 512u);
    const srd_t sK  = __builtin_amdgcn_make_buffer_rsrc((void*)(pk + kvoff), 0, 524288, 0x00020000);
    const srd_t sV  = __builtin_amdgcn_make_buffer_rsrc((void*)(pv + kvoff), 0, 524288, 0x00020000);
    const srd_t sWo = __builtin_amdgcn_make_buffer_rsrc((void*)(pwo + mem * (256u * 896u)), 0, 256 * 896, 0x00020000);
    unsigned vK[4];
    { const unsigned kin = hi * 512u + (((l & 31u) ^ hi) * 16u);
      #pragma unroll
      for (int i = 0; i < 4; ++i) vK[i] = w * 4096u + (kin ^ (((4u * w + i) & 7u) * 32u)); }
    const unsigned vV = (8u * w + row) * 512u + col;
    const unsigned vWo = (32u * w + row) * 896u + col;
    const unsigned krot = ROTK ? (((grp * 8u + kvh) * 2654435761u) >> 19) & 31u : 0u;
    auto tile_of = [&](unsigned p, bool& nt) -> unsigned {
      if constexpr (SWP == 0) { nt = LOCK_NT; return (p + krot) & 31u; }
      else {
        const bool sw_ = (((p / (2u * SWP)) + b) % MIXPER) < MIXN;
        nt = sw_ ? ((p & SWP) != 0u) : true;
        return ((sw_ ? (p ^ (hs ? SWP : 0u)) : p) + krot) & 31u;
      }
    };
    auto KH = [&](auto hh, unsigned p, unsigned n) { constexpr int H = decltype(hh)::value; bool nt; const unsigned t = tile_of(p, nt); const unsigned m = wb + ((n + 2u) & 7u) * 16384u;
      if (nt) dma_k<H, true>(m, vK, sK, t * 16384u); else dma_k<H, false>(m, vK, sK, t * 16384u); };
    auto VH = [&](auto hh, unsigned p, unsigned n) { constexpr int H = decltype(hh)::value; bool nt; const unsigned t = tile_of(p, nt); const unsigned m = wb + ((n + 2u) & 7u) * 16384u;
      if (nt) dma_cols<H, true>(m, vV, sV, t * 16384u); else dma_cols<H, false>(m, vV, sV, t * 16384u); };
    unsigned co = rot * 128u;
    auto OA = [&](unsigned i) { if (!NOWO || i < 3) dma_wo<0>(wb + wo_slot(i) * 16384u, vWo, sWo, co + (i & 1u) * (128u * 896u)); };
    auto OB = [&](unsigned i) { if (!NOWO || i < 3) dma_wo<1>(wb + wo_slot(i) * 16384u, vWo, sWo, co + (i & 1u) * (128u * 896u)); if (i & 1u) { co += 128u; co = co == 896u ? 0u : co; } };
    constexpr std::integral_constant<int, 0> A{}; constexpr std::integral_constant<int, 1> B{};
    asm volatile("s_waitcnt lgkmcnt(0)" : "+s"(epoch) :: "memory");
    vmwait<8>(); lbar();
    WA(4); vmwait<DBH>(); WB(4);
    unsigned sl = 5;
    #pragma nounroll
    for (int s = 1; s < 5; ++s) {
      vmwait<8>(); WA(sl); vmwait<DBH>(); WB(sl); sl = sl == 5u ? 0u : sl + 1u;
      vmwait<8>(); lbar();
      WA(sl); vmwait<DBH>(); WB(sl); sl = sl == 5u ? 0u : sl + 1u;
    }
    vmwait<8>(); WA(sl); vmwait<DBH>(); WB(sl);
    vmwait<8>(); lbar();
    KH(A, 0, 0); vmwait<DB>(); KH(B, 0, 0);
    vmwait<8>(); KH(A, 1, 1); vmwait<DB>(); KH(B, 1, 1);
    vmwait<8>(); lbar();
    KH(A, 2, 2);
    lbar();
    vmwait<DB>(); KH(B, 2, 2);
    vmwait<8>(); KH(A, 3, 3); vmwait<DB>(); KH(B, 3, 3);
    vmwait<8>(); KH(A, 4, 4); lbar();
    vmwait<DB>(); KH(B, 4, 4);
    vmwait<8>(); VH(A, 0, 5); lbar();
    vmwait<DB>(); VH(B, 0, 5);
    vmwait<8>(); KH(A, 5, 6); lbar();
    vmwait<DB>(); KH(B, 5, 6);
    unsigned sdummy = 0;
    #pragma nounroll
    for (unsigned p = 3; p < 29u; ++p) {
      vmwait<8>(); VH(A, p - 2u, 2u * p + 1u); vmwait<DB>(); VH(B, p - 2u, 2u * p + 1u);
      vmwait<8>(); KH(A, p + 3u, 2u * p + 2u); lbar();
      vmwait<DB>(); KH(B, p + 3u, 2u * p + 2u);
    }
    asm volatile("s_waitcnt lgkmcnt(0)" : "+s"(sdummy) :: "memory");
    #pragma nounroll
    for (unsigned p = 29; p < 31u; ++p) {
      vmwait<8>(); VH(A, 2u * p - 31u, 2u * p + 1u); vmwait<DB>(); VH(B, 2u * p - 31u, 2u * p + 1u);
      vmwait<8>(); VH(A, 2u * p - 30u, 2u * p + 2u); lbar();
      vmwait<DB>(); VH(B, 2u * p - 30u, 2u * p + 2u);
    }
    vmwait<8>(); VH(A, 31, 63); lbar(); vmwait<DB>(); VH(B, 31, 63);
    vmwait<8>(); OA(0); lbar(); vmwait<DBH>(); OB(0);
    vmwait<8>(); OA(1); lbar(); vmwait<DBH>(); OB(1);
    vmwait<8>(); OA(2); lbar();
    vmwait<DBH>(); OB(2);
    vmwait<8>(); OA(3); vmwait<DBH>(); OB(3);
    #pragma unroll
    for (unsigned c = 0; c < 5u; ++c) {
      vmwait<8>(); OA(2u * c + 4u); if (!COFF) lbar(); vmwait<DBH>(); OB(2u * c + 4u);
      vmwait<8>(); OA(2u * c + 5u); if (COFF) lbar(); vmwait<DBH>(); OB(2u * c + 5u);
    }
    vmwait<8>(); lbar();
    vmwait<4>(); lbar();
    vmwait<0>(); lbar();
    tag = epoch + 0x9E3779B97F4A7C15ull;
  } else {
  CST(0);
  u64 epoch;
  asm volatile("s_load_dwordx2 %0, %1, %2\n\ts_waitcnt lgkmcnt(0)" : "=s"(epoch) : "s"(ws), "s"(grp * 256u) : "memory");
  tag = epoch + 0x9E3779B97F4A7C15ull;
  {
    const u64 pc = pc0;
    unsigned d = 0, o = ((j & 3u) * 4u + w) * 64u;
    unsigned len; asm volatile("s_mov_b32 %0, .Lgqa_end-.Lgqa_start" : "=s"(len));
    #pragma nounroll
    for (; o < len; o += 1024u) asm volatile("s_load_dword %0, %1, %2" : "+s"(d) : "s"(pc), "s"(o) : "memory");
    asm volatile("s_waitcnt lgkmcnt(0)" : "+s"(d) :: "memory");
  }
  const unsigned q16 = l & 15u, g = l >> 4, q32 = l & 31u, h = l >> 5, tp = l & 3u, tr = (l >> 2) & 3u;
  const unsigned perm = 8u * (4u * (tp & 1u) + 2u * (g & 1u) + (tp >> 1));
  f32x16 zero;
  #pragma unroll
  for (int e = 0; e < 16; ++e) zero[e] = 0.f;

  bf16x8 qf[8];
  {
    f32x16 acc[2][2];
    const unsigned wqb = ldsb + h * 4096u + w * 1024u + tr * 64u + perm;
    const unsigned wq2 = opq(wqb + 32768u);
    const unsigned xsw = (q32 >> 1) & 7u;
    const unsigned xb = ldsb + L_XST + (q32 >> 3) * 7168u + (q32 & 7u) * 128u;
    unsigned xo[4];
    #pragma unroll
    for (int t = 0; t < 4; ++t) xo[t] = xb + (((2u * t + h) ^ xsw) * 16u);
    const unsigned qw = ldsb + L_EXT + q32 * 512u;
    unsigned kb = rot;
    bf16x8 xf[2][4][2], wf[2][4][2];
    #pragma unroll
    for (int s = 0; s < 7; ++s) {
      cbar();
      if (s == 0) CST(1);
      const unsigned a = wqb + ((2 * s) % 6) * 16384u, xk = kb * 1024u;
      #pragma unroll
      for (int t = 0; t < 4; ++t) {
        #pragma unroll
        for (int z = 0; z < 4; ++z) {
          if (s > 0) acc[z >> 1][z & 1] = mfma32(wf[(s - 1) & 1][t][z >> 1], xf[(s - 1) & 1][t][z & 1], (s == 1 && t == 0) ? zero : acc[z >> 1][z & 1]);
          if (t < 3) {
            const int f0 = (t * 4 + z) * 3 / 2, f1 = (t * 4 + z + 1) * 3 / 2;
            #pragma unroll
            for (int f = f0; f < f1 && f < 16; ++f) {
              const int tt = f >> 2, k = f & 3;
              if (k < 2) { const unsigned aa = (((2 * s) % 6) >= 2 ? wq2 + (((2 * s) % 6) - 2) * 16384u : a) + tt * 8192u + k * 512u; wf[s & 1][tt][k] = trfrag(aa, aa + 256u); }
              else xf[s & 1][tt][k - 2] = ldsfrag(xo[tt] + xk + (k - 2) * (4u * 7168u));
            }
          }
          PIN();
        }
      }
      kb = kb == 6u ? 0u : kb + 1u;
    }
    cbar();
    CST(2);
    #pragma unroll
    for (int z = 0; z < 5; ++z) {
      #pragma unroll
      for (int t = 0; t < 4; ++t) {
        if (z < 4) acc[z >> 1][z & 1] = mfma32(wf[0][t][z >> 1], xf[0][t][z & 1], acc[z >> 1][z & 1]);
        if (z > 0 && (t & 1)) {
          const int y = z - 1, i = t >> 1;
          const f32x16& a = acc[y >> 1][y & 1];
          u32x4 v = {cvtpk(a[8 * i], a[8 * i + 1]), cvtpk(a[8 * i + 2], a[8 * i + 3]), cvtpk(a[8 * i + 4], a[8 * i + 5]), cvtpk(a[8 * i + 6], a[8 * i + 7])};
          *(LAS u32x4*)(size_t)(qw + (y & 1) * 16384u + (((8u * w + 4u * (y >> 1) + 2u * h + i) ^ q16) * 16u)) = v;
        }
        PIN();
      }
    }
    CST(3);
  }
  STAMP_AT(1);
  f32x16 O[2][2];
  #pragma unroll
  for (int i = 0; i < 4; ++i) O[i >> 1][i & 1] = zero;
  unsigned kb[4];
  #pragma unroll
  for (int u = 0; u < 4; ++u) kb[u] = ldsb + q16 * 512u + (((4u * u + g) ^ q16) * 16u);
  const unsigned vb = ldsb + w * 1024u + (4u * h + tr) * 64u + 32u * (g & 1u) + 8u * tp;
  const unsigned pw = ldsb + L_EXT + w * 8192u + l * 16u;
  const unsigned pr = ldsb + L_EXT + (g & 1u) * 8192u + (h * 16u + q16) * 16u;
  const unsigned aw = ldsb + L_EXT + w * 8192u + L_AL + q16 * 4u, ar = ldsb + L_EXT + (g & 1u) * 8192u + L_AL + q16 * 4u;
  const float cs = 0.0625f * 1.4426950408889634f;
  float mref = 0.f, lsum = 0.f;

  struct PVF { bf16x8 pf[2][2], vf[2][2]; float a0, a1; };
  auto pv_read = [&](PVF& f, int i, unsigned va, unsigned pa) {
    const int ks = i >> 2, k = i & 3;
    if (k == 0) f.vf[0][ks] = trfrag(va + ks * 4096u, va + ks * 4096u + 8192u);
    if (k == 1) f.pf[0][ks] = ldsfrag(pa + ks * 512u);
    if (k == 2) f.pf[1][ks] = ldsfrag(pa + 16384u + ks * 512u);
    if (k == 3) f.vf[1][ks] = trfrag(va + ks * 4096u + 512u, va + ks * 4096u + 512u + 8192u);
  };
  auto pv_m = [&](const PVF& f, int i) { const int ks = i >> 2, db = (i >> 1) & 1, qb = i & 1; O[db][qb] = mfma32(f.vf[db][ks], f.pf[qb][ks], O[db][qb]); };
  auto rescale = [&](float a0, float a1) {
    if (__builtin_expect(__any(a0 != 1.f || a1 != 1.f), 0)) {
      #pragma unroll
      for (int db = 0; db < 2; ++db)
        #pragma unroll
        for (int e = 0; e < 16; ++e) { O[db][0][e] *= a0; O[db][1][e] *= a1; }
    }
  };
  bf16x8 kf[16];
  auto k_read = [&](int i, const unsigned (&kk)[4]) { kf[i] = ldsfrag(kk[(i >> 1) & 3] + (i >> 3) * 256u + (i & 1) * 8192u); };
  auto phase = [&](auto mode, unsigned kslot, unsigned vslot, unsigned p) {
    constexpr int MODE = decltype(mode)::value;
    constexpr bool FIRST = MODE == 0, PV = MODE >= 2, LAST = MODE == 3;
    PVF f;
    unsigned kk[4], va = 0, pa = 0;
    if constexpr (PV) {
      const unsigned rr = (p + 1u) & 3u;
      va = opq(vb + vslot); pa = opq(pr + rr * 1024u);
      const unsigned aa = ar + rr * 64u; f.a0 = ldsf(aa); f.a1 = ldsf(aa + 16384u);
    }
    PIN();
    f32x4 s0, s1;
    #pragma unroll
    for (int i = 0; i < 16; ++i) {
      if (i == 0) s0 = mfma16(kf[0], qf[0], f32x4{0.f, 0.f, 0.f, 0.f});
      else if (i == 1) s1 = mfma16(kf[1], qf[0], f32x4{0.f, 0.f, 0.f, 0.f});
      else if (i & 1) s1 = mfma16(kf[i], qf[i >> 1], s1);
      else s0 = mfma16(kf[i], qf[i >> 1], s0);
      if constexpr (PV) {
        if (i < 8) pv_read(f, i, va, pa);
        if (!LAST && i >= 8 && i < 12) kk[i - 8] = opq(kb[i - 8] + kslot);
        if (!LAST && i >= 12) k_read(i - 12, kk);
      } else {
        if (i < 4) kk[i] = opq(kb[i] + kslot);
        else k_read(i - 4, kk);
      }
      PIN();
    }
    constexpr int K0 = PV ? 4 : 12;
    if constexpr (PV) { rescale(f.a0, f.a1); PIN(); pv_m(f, 0); }
    if constexpr (!LAST) { k_read(K0, kk); k_read(K0 + 1, kk); }
    PIN();
    float t[8];
    #pragma unroll
    for (int e = 0; e < 4; ++e) { t[e] = __builtin_fmaf(s0[e], cs, -mref); t[4 + e] = __builtin_fmaf(s1[e], cs, -mref); }
    PIN();
    if constexpr (PV) pv_m(f, 1);
    if constexpr (!LAST) { k_read(K0 + 2, kk); k_read(K0 + 3, kk); }
    PIN();
    float mx = fmaxf(fmaxf(fmaxf(t[0], t[1]), t[2]), fmaxf(fmaxf(fmaxf(t[3], t[4]), t[5]), fmaxf(t[6], t[7])));
    float alpha = 1.f;
    if (FIRST || __builtin_expect(__any(mx > THR), 0)) {
      auto r = __builtin_amdgcn_permlane16_swap(__float_as_uint(mx), __float_as_uint(mx), false, false);
      mx = fmaxf(__uint_as_float(r[0]), __uint_as_float(r[1]));
      r = __builtin_amdgcn_permlane32_swap(__float_as_uint(mx), __float_as_uint(mx), false, false);
      mx = fmaxf(__uint_as_float(r[0]), __uint_as_float(r[1]));
      if constexpr (!FIRST) { mx = fmaxf(mx, 0.f); alpha = __builtin_amdgcn_exp2f(-mx); lsum *= alpha; }
      mref += mx;
      #pragma unroll
      for (int e = 0; e < 8; ++e) t[e] -= mx;
    }
    PIN();
    #pragma unroll
    for (int z = 0; z < 3; ++z) {
      if constexpr (PV) pv_m(f, 2 + z);
      if constexpr (PV && !LAST) { k_read(8 + 2 * z, kk); k_read(9 + 2 * z, kk); }
      PIN();
      #pragma unroll
      for (int e = 3 * z; e < 3 * z + 3 && e < 8; ++e) { t[e] = __builtin_amdgcn_exp2f(t[e]); asm volatile("" : "+v"(t[e])); }
      PIN();
    }
    if constexpr (PV) pv_m(f, 5);
    if constexpr (PV && !LAST) { k_read(14, kk); k_read(15, kk); }
    PIN();
    if constexpr (LAST) {
      lsum += ((t[0] + t[1]) + (t[2] + t[3])) + ((t[4] + t[5]) + (t[6] + t[7]));
      auto r = __builtin_amdgcn_permlane16_swap(__float_as_uint(lsum), __float_as_uint(lsum), false, false);
      lsum = __uint_as_float(r[0]) + __uint_as_float(r[1]);
      r = __builtin_amdgcn_permlane32_swap(__float_as_uint(lsum), __float_as_uint(lsum), false, false);
      const float li = __builtin_amdgcn_rcpf(__uint_as_float(r[0]) + __uint_as_float(r[1]));
      PIN(); pv_m(f, 6); PIN();
      alpha *= li;
      #pragma unroll
      for (int e = 0; e < 8; ++e) t[e] *= li;
    }
    u32x4 v = {cvtpk(t[0], t[1]), cvtpk(t[2], t[3]), cvtpk(t[4], t[5]), cvtpk(t[6], t[7])};
    *(LAS u32x4*)(size_t)(pw + (p & 3u) * 1024u) = v;
    *(LAS float*)(size_t)(aw + (p & 3u) * 64u) = alpha;
    PIN();
    if constexpr (!LAST) {
      if constexpr (PV) { pv_m(f, 6); PIN(); }
      lsum += ((t[0] + t[1]) + (t[2] + t[3])) + ((t[4] + t[5]) + (t[6] + t[7]));
    }
    PIN();
    if constexpr (PV) { pv_m(f, 7); PIN(); }
  };

  CST(4);
  cbar();
  CST(5);
  {
    unsigned kk[4];
    #pragma unroll
    for (int u = 0; u < 4; ++u) kk[u] = opq(kb[u] + 2u * 16384u);
    #pragma unroll
    for (int u = 0; u < 8; ++u) { qf[u] = ldsfrag(kb[u & 3] + L_EXT + w * 8192u + (u >> 2) * 256u); k_read(2 * u, kk); k_read(2 * u + 1, kk); PIN(); }
  }
  CST(6);
  phase(std::integral_constant<int, 0>{}, 3u * 16384u, 0u, 0u);
  CST(7);
  #pragma nounroll
  for (unsigned p = 1; p < 3u; ++p) { cbar(); phase(std::integral_constant<int, 1>{}, (p + 3u) * 16384u, 0u, p); }
  #pragma nounroll
  for (unsigned p = 3; p < 31u; ++p) {
    cbar();
    phase(std::integral_constant<int, 2>{}, ((2u * p) & 7u) * 16384u, ((2u * p + 1u) & 7u) * 16384u, p);
  }
  cbar();
  CST(8);
  phase(std::integral_constant<int, 3>{}, 0u, 6u * 16384u, 31u);
  const unsigned ost = ldsb + L_EXT + l * 16u;
  const unsigned wob = ldsb + (w & 1u) * 512u + (4u * h + tr) * 64u + perm;
  const unsigned pst = WS_PART + (grp * 16u + mem) * (7u * 8192u) + w * 2048u + l * 16u;
  const unsigned fl = l == 0 ? WS_FLAG + ((grp * 4u + w) * 16u + mem) * FS : 0x7fffffffu;
  {
    PVF f0, f1, f;
    auto pre = [&](PVF& d, unsigned rr) {
      d.a0 = ldsf(ar + rr * 64u); d.a1 = ldsf(ar + rr * 64u + 16384u);
      #pragma unroll
      for (int i = 0; i < 8; ++i) if ((i & 3) == 1 || (i & 3) == 2) pv_read(d, i, 0u, pr + rr * 1024u);
      PIN();
    };
    auto vrd = [&](PVF& d, unsigned va) {
      #pragma unroll
      for (int i = 0; i < 4; ++i) { pv_read(d, (i >> 1) * 4 + (i & 1) * 3, va, 0u); PIN(); }
    };
    const unsigned v7 = opq(vb + 7u * 16384u), ow = opq(ost + w * 4096u);
    pre(f0, 1u); pre(f1, 2u);
    asm volatile("" :: "v"(wob), "v"(pst), "v"(fl));
    rescale(f0.a0, f0.a1);
    cbar();
    vrd(f0, v7); pre(f, 3u);
    #pragma unroll
    for (int i = 0; i < 8; ++i) { pv_m(f0, i); PIN(); }
    rescale(f1.a0, f1.a1);
    cbar();
    vrd(f1, vb);
    #pragma unroll
    for (int i = 0; i < 8; ++i) { pv_m(f1, (i >> 1) + 4 * (i & 1)); PIN();
      if (i >= 3 && (i & 1)) { const int z = (i - 3) >> 1; O[z >> 1][z & 1] *= (z & 1) ? f.a1 : f.a0; asm volatile("" : "+v"(O[z >> 1][z & 1])); PIN(); } }
    O[1][1] *= f.a1; asm volatile("" : "+v"(O[1][1]));
    CST(9);
    STAMP_AT(2);
    cbar();
    CST(10);
    STAMP_AT(3);
    #pragma unroll
    for (int i = 0; i < 4; ++i) { pv_read(f, (i & 1) * 4 + (i >> 1) * 3, vb + 16384u, 0u); PIN(); }
    pv_m(f, 0); pv_m(f, 4); PIN();
    CST(11);
    #pragma unroll
    for (int z = 0; z < 4; ++z) {
      const f32x16& a = O[z >> 1][z & 1];
      #pragma unroll
      for (int i = 0; i < 2; ++i) {
        if (z < 3) { pv_m(f, z + 1 + 4 * i); PIN(); }
        u32x4 v = {cvtpk(a[8 * i], a[8 * i + 1]), cvtpk(a[8 * i + 2], a[8 * i + 3]), cvtpk(a[8 * i + 4], a[8 * i + 5]), cvtpk(a[8 * i + 6], a[8 * i + 7])};
        *(LAS u32x4*)(size_t)(ow + (z & 1) * 16384u + (2u * (z >> 1) + i) * 1024u) = v;
        PIN();
      }
    }
  }
  CST(12);
  STAMP_AT(4);
  {
    bf16x8 of[16];
    const u32x2 tg = {(unsigned)tag, (unsigned)(tag >> 32)};
    unsigned ch = rot;
    bf16x8 fr[8];
    f32x16 acc[2];
    u32x4 res[2];
    auto pack = [&](int c, int k) {
      const f32x16& a = acc[c & 1];
      res[k >> 2][k & 3] = cvtpk(a[2 * k], a[2 * k + 1]);
      asm volatile("" : "+v"(res[k >> 2]));
    };
    auto put = [&](int i) { if (NOST) asm volatile("" :: "v"(res[i])); else __builtin_amdgcn_raw_buffer_store_b128(res[i], sWs, pst + i * 1024u, ch * 8192u, 16); if (i) ch = ch == 6u ? 0u : ch + 1u; };
    const unsigned ord = ost + (w >> 1) * 16384u;
    unsigned alast = 0;
    #pragma unroll
    for (int c = 0; c < 8; ++c) {
      unsigned a = 0;
      if (c < 7) {
        if (c <= 2) cbar(); else { PIN(); asm volatile("s_barrier" ::: "memory"); PIN(); }
        if (c == 0) CST(13);
        if (c == 6) CST(14);
        a = opq(wob + wo_slot(2 * c) * 16384u);
      }
      if (c == 7) {
        PIN(); asm volatile("s_barrier" ::: "memory"); PIN();
        #pragma unroll
        for (int s = 8; s < 16; ++s) { fr[s & 7] = trfrag(alast + s * 2048u, alast + s * 2048u + 1024u); PIN(); }
      }
      alast = a;
      #pragma unroll
      for (int s = 0; s < 16; ++s) {
        if (s < 8) { if (c > 0) acc[(c - 1) & 1] = mfma32(fr[s], of[8 + s], acc[(c - 1) & 1]); }
        else if (c < 7) acc[c & 1] = mfma32(fr[s & 7], of[s - 8], s == 8 ? zero : acc[c & 1]);
        PIN();
        if (c == 0) of[s] = ldsfrag(ord + s * 1024u);
        if (c < 6 || (c == 6 && s < 8)) fr[s & 7] = trfrag(a + s * 2048u, a + s * 2048u + 1024u);
        PIN();
        if (c > 0 && (s == 10 || s == 11 || s == 13 || s == 14)) { const int k = s < 12 ? 2 * (s - 10) : 2 * (s - 11); pack(c - 1, k); pack(c - 1, k + 1); PIN(); }
        if (c > 0 && (s == 12 || s == 15)) { put(s == 15); PIN(); }
      }
    }
    CST(15);
    vmwait<0>();
    CST(16);
    __builtin_amdgcn_raw_buffer_store_b64(tg, sWs, fl, 0, 16);
  }
  asm volatile(".Lgqa_end:");
  STAMP_AT(5);
  }
  if (w8 < 7u) {
    reduce_chunk(sWs, out, grp, mem, w8, l, b, br, tag);
    if (w8 == 0u && mem == 0u) {
      u32x2 t = {(unsigned)tag, (unsigned)(tag >> 32)};
      __builtin_amdgcn_raw_buffer_store_b64(t, sWs, l == 0 ? WS_EPOCH + grp * 256u : 0x7fffffffu, 0, 16);
    }
  }
  STAMP_AT(6);
}

extern "C" void kernel_launch(void* const* d_in, const int* in_sizes, int n_in, void* d_out, int out_size, void* d_ws, size_t ws_size, hipStream_t stream) {
  (void)in_sizes; (void)n_in; (void)out_size; (void)ws_size;
  (void)hipFuncSetAttribute((const void*)gqa2_fused, hipFuncAttributeMaxDynamicSharedMemorySize, 163840);
  Args a;
  for (int i = 0; i < 10; ++i) a.in[i] = (const char*)d_in[i];
  a.out = (char*)d_out; a.ws = (char*)d_ws;
  hipLaunchKernelGGL(gqa2_fused, dim3(256), dim3(512), 163840, stream, a);
}
```

```cpp
#include <hip/hip_runtime.h>
#include <type_traits>

typedef __bf16   bf16x8 __attribute__((ext_vector_type(8)));
typedef __bf16   bf16x4 __attribute__((ext_vector_type(4)));
typedef __bf16   bf16x2 __attribute__((ext_vector_type(2)));
typedef float    f32x4  __attribute__((ext_vector_type(4)));
typedef float    f32x16 __attribute__((ext_vector_type(16)));
typedef float    f32x2  __attribute__((ext_vector_type(2)));
typedef unsigned u32x4  __attribute__((ext_vector_type(4)));
typedef unsigned u32x2  __attribute__((ext_vector_type(2)));
typedef unsigned long long u64;
typedef __amdgpu_buffer_rsrc_t srd_t;
#define LAS __attribute__((address_space(3)))
#define DI __device__ __forceinline__
#define PIN() __builtin_amdgcn_sched_barrier(0)

#define STAMP 0
#define SWP 0
#define MIXPER 1
#define MIXN 1
#define LOCK_NT 1
#define CODEPF 1
#define ROTK 1
#define WOPF 0
#define PRETOUCH 0
#define NOWO 0
#define NOST 0
#define LAUX 16
#define COFF 1
#define DEEP 0
#define NOCOMP 0
#define KPAT 0
#define NOBAR 0
#define RUNA 0
#define RUNE 0
#define WOT 0
#define NOKV 0
#define BAL 1
#define RED 0
#define EARLY 0
#define NORED 0
#define POLL 1
#define PGAP 2
#define PMODE 0
#define PWAVE 7
#define HEAD2 1
#define DBH 8
#define DB 8

constexpr unsigned WS_EPOCH = 0;
#define FS 8
constexpr unsigned WS_FLAG  = 4096;
constexpr unsigned WS_PART  = 1u << 20;
constexpr unsigned WS_PART_BYTES = 256u * 7u * 8192u;
constexpr unsigned WS_END   = WS_PART + WS_PART_BYTES;
constexpr unsigned WS_STAMP = 32u << 20;
constexpr int NSTAMP = 7, NBAR = 51, LW0 = 0;
constexpr unsigned L_XST = 163840u - 57344u;
constexpr unsigned L_EXT = 131072u;
constexpr unsigned L_AL  = 4096u;
constexpr float THR = 8.0f;
constexpr unsigned wo_slot(unsigned i) { return i < 8u ? (i + 2u) & 7u : i < 10u ? i : i - 8u; }

struct Args { const char* in[10]; char* out; char* ws; };

DI unsigned cvtpk(float lo, float hi) { f32x2 v = {lo, hi}; bf16x2 b = __builtin_convertvector(v, bf16x2); return __builtin_bit_cast(unsigned, b); }
DI f32x4 mfma16(bf16x8 a, bf16x8 b, f32x4 c) { return __builtin_amdgcn_mfma_f32_16x16x32_bf16(a, b, c, 0, 0, 0); }
DI f32x16 mfma32(bf16x8 a, bf16x8 b, f32x16 c) { return __builtin_amdgcn_mfma_f32_32x32x16_bf16(a, b, c, 0, 0, 0); }
DI bf16x8 trfrag(unsigned a0, unsigned a1) {
  bf16x4 lo = __builtin_amdgcn_ds_read_tr16_b64_v4bf16((LAS bf16x4*)(size_t)a0);
  bf16x4 hi = __builtin_amdgcn_ds_read_tr16_b64_v4bf16((LAS bf16x4*)(size_t)a1);
  return __builtin_shufflevector(lo, hi, 0, 1, 2, 3, 4, 5, 6, 7);
}
DI unsigned opq(unsigned a) { asm volatile("" : "+v"(a)); return a; }
DI bf16x8 ldsfrag(unsigned a) { return *(const LAS bf16x8*)(size_t)a; }
DI float ldsf(unsigned a) { return *(const LAS float*)(size_t)a; }
DI void cbar() { PIN(); asm volatile("s_waitcnt lgkmcnt(0)\n\ts_barrier" ::: "memory"); PIN(); }
DI void lbar() { asm volatile("s_barrier" ::: "memory"); }
template <int N> DI void vmwait() { asm volatile("s_waitcnt vmcnt(%0)" :: "i"(N) : "memory"); }

template <int H, bool NT> DI void dma_cols(unsigned m, unsigned v, srd_t srd, unsigned so) {
  if constexpr (NT)
    asm volatile("s_add_u32 m0, %0, %4\n\ts_nop 0\n\tbuffer_load_dwordx4 %1, %2, %3 offen offset:%5 nt lds\n\t"
                 "s_add_u32 m0, %0, %6\n\ts_nop 0\n\tbuffer_load_dwordx4 %1, %2, %3 offen offset:%7 nt lds"
                 :: "s"(m), "v"(v), "s"(srd), "s"(so), "i"(H * 1792), "i"(H * 256), "i"(H * 1792 + 896), "i"(H * 256 + 128) : "memory", "scc");
  else
    asm volatile("s_add_u32 m0, %0, %4\n\ts_nop 0\n\tbuffer_load_dwordx4 %1, %2, %3 offen offset:%5 lds\n\t"
                 "s_add_u32 m0, %0, %6\n\ts_nop 0\n\tbuffer_load_dwordx4 %1, %2, %3 offen offset:%7 lds"
                 :: "s"(m), "v"(v), "s"(srd), "s"(so), "i"(H * 1792), "i"(H * 256), "i"(H * 1792 + 896), "i"(H * 256 + 128) : "memory", "scc");
}
template <int H, bool NT> DI void dma_k(unsigned m, const unsigned (&v)[4], srd_t srd, unsigned so) {
  if constexpr (NT)
    asm volatile("s_mov_b32 m0, %0\n\ts_nop 0\n\tbuffer_load_dwordx4 %1, %3, %4 offen offset:%5 nt lds\n\tbuffer_load_dwordx4 %2, %3, %4 offen offset:%6 nt lds"
                 :: "s"(m), "v"(v[2 * H]), "v"(v[2 * H + 1]), "s"(srd), "s"(so), "i"(H * 2048), "i"(H * 2048 + 1024) : "memory");
  else
    asm volatile("s_mov_b32 m0, %0\n\ts_nop 0\n\tbuffer_load_dwordx4 %1, %3, %4 offen offset:%5 lds\n\tbuffer_load_dwordx4 %2, %3, %4 offen offset:%6 lds"
                 :: "s"(m), "v"(v[2 * H]), "v"(v[2 * H + 1]), "s"(srd), "s"(so), "i"(H * 2048), "i"(H * 2048 + 1024) : "memory");
}
template <int H> DI void dma_wo(unsigned m, unsigned v, srd_t srd, unsigned so) {
  unsigned t;
  asm volatile("s_add_u32 m0, %1, %5\n\ts_add_u32 %0, %4, %6\n\tbuffer_load_dwordx4 %2, %3, %0 offen lds\n\t"
               "s_add_u32 m0, %1, %7\n\ts_add_u32 %0, %4, %8\n\tbuffer_load_dwordx4 %2, %3, %0 offen lds"
               : "=&s"(t) : "s"(m), "v"(v), "s"(srd), "s"(so), "i"(H * 2048), "i"(H * 14336), "i"(H * 2048 + 1024), "i"(H * 14336 + 7168) : "memory", "scc");
}
DI void dma_one(unsigned m, unsigned v, srd_t srd, unsigned so) {
  asm volatile("s_mov_b32 m0, %0\n\ts_nop 0\n\tbuffer_load_dwordx4 %1, %2, %3 offen lds" :: "s"(m), "v"(v), "s"(srd), "s"(so) : "memory");
}

#define STAMP_AT(i) do {} while (0)
#define CST(i) do {} while (0)

#define PSLEEP 1
DI void reduce_chunk(srd_t sWs, char* out, unsigned grp, unsigned mem, unsigned ch, unsigned l, unsigned b, unsigned br, u64 tag, unsigned early, bool poller) {
  const unsigned fa = WS_FLAG + early * (1024u * FS) + ((grp * 4u + (mem >> 2)) * 16u + (l & 15u)) * FS;
  const unsigned pa = WS_PART + (grp * 16u + (l >> 5)) * (7u * 8192u) + ch * 8192u + mem * 512u + (l & 31u) * 16u;
  const unsigned wv = mem >> 2, q = 32u * (wv >> 1) + (l & 31u), n = 64u * ch + 32u * (wv & 1u) + 16u * (mem & 1u) + 8u * ((mem >> 1) & 1u) + 4u * (l >> 5);
  char* const po = out + (size_t)(b * 64u + q) * 1792u + br * 896u + n * 2u;
  asm volatile("" :: "v"(po), "v"(pa));
  u32x2 pend[POLL];
  #pragma unroll
  for (int i = 0; i < POLL; ++i) pend[i] = u32x2{0u, 0u};
  if (poller) {
    #pragma nounroll
    for (unsigned spins = 0; spins < (1u << 20); ++spins) {
      u32x2 fv = {0u, 0u};
      asm volatile("buffer_load_dwordx2 %0, %1, %2, 0 offen sc1\n\ts_waitcnt vmcnt(0)" : "+v"(fv) : "v"(fa), "s"(sWs) : "memory");
      if ((__ballot(fv[0] == (unsigned)tag && fv[1] == (unsigned)(tag >> 32)) & 0xffffu) == 0xffffu) break;
      if (PGAP) __builtin_amdgcn_s_sleep(PGAP);
    }
  }
  asm volatile("s_barrier" ::: "memory");
  if (ch >= 7u) return;
  #pragma nounroll
  for (unsigned spins = 0; spins < ((NORED == 2 || POLL) ? 0u : 1u << 20); ++spins) {
    u32x2 fv;
    asm volatile("buffer_load_dwordx2 %0, %1, %2, 0 offen sc1\n\ts_waitcnt vmcnt(0)" : "=v"(fv) : "v"(fa), "s"(sWs) : "memory");
    if (__all(fv[0] == (unsigned)tag && fv[1] == (unsigned)(tag >> 32))) break;
    if (PSLEEP) __builtin_amdgcn_s_sleep(PSLEEP);
  }
  u32x4 r[8];
  #pragma unroll
  for (int i = 0; i < 8; ++i) r[i] = __builtin_amdgcn_raw_buffer_load_b128(sWs, (NORED == 1 || NORED == 2 || (NORED == 3 && i >= 4) || (NORED == 4 && i >= 1)) ? 0x7ffffff0u : pa, i * (2u * 7u * 8192u), LAUX);
  float a[8];
  #pragma unroll
  for (int i = 0; i < 8; ++i) {
    #pragma unroll
    for (int e = 0; e < 4; ++e) {
      const float lo = __uint_as_float(r[i][e] << 16), hi = __uint_as_float(r[i][e] & 0xffff0000u);
      a[2 * e] = i ? a[2 * e] + lo : lo; a[2 * e + 1] = i ? a[2 * e + 1] + hi : hi;
    }
    PIN();
  }
  #pragma unroll
  for (int i = 0; i < POLL; ++i) asm volatile("" :: "v"(pend[i]));
  float o[4];
  #pragma unroll
  for (int e = 0; e < 4; ++e) {
    auto s = __builtin_amdgcn_permlane32_swap(__float_as_uint(a[e]), __float_as_uint(a[4 + e]), false, false);
    o[e] = __uint_as_float(s[0]) + __uint_as_float(s[1]);
  }
  *(u32x2*)po = u32x2{cvtpk(o[0], o[1]), cvtpk(o[2], o[3])};
}

__global__ __launch_bounds__(512, 2)
void gqa2_fused(Args args) {
  extern __shared__ __attribute__((aligned(16))) char lds[];
  u64 pc0; asm volatile("s_getpc_b64 %0\n.Lgqa_start:" : "=s"(pc0));
  const unsigned bid = blockIdx.x, br = (bid >> 2) & 1u, j = bid >> 3, hs = j & 1u, b = j >> 2;
  const char* const px = args.in[br * 5u], * const pk = args.in[br * 5u + 1u], * const pv = args.in[br * 5u + 2u], * const pwq = args.in[br * 5u + 3u], * const pwo = args.in[br * 5u + 4u];
  char* const out = args.out; char* const ws = args.ws;
  asm volatile("" :: "s"(px), "s"(pk), "s"(pv), "s"(pwq), "s"(pwo), "s"(out), "s"(ws));
  const unsigned kvh = (bid & 3u) * 2u + ((j >> 1) & 1u), grp = br * 8u + b, mem = kvh * 2u + hs;
  const unsigned tid = threadIdx.x, l = tid & 63u;
  const unsigned w8 = __builtin_amdgcn_readfirstlane(tid >> 6), w = w8 & 3u;
  const unsigned rot = b == 7u ? 3u : b;
  const unsigned ldsb = (unsigned)(size_t)lds;
  STAMP_AT(0);
  const srd_t sWs = __builtin_amdgcn_make_buffer_rsrc((void*)ws, 0, WS_END, 0x00020000);

  u64 tag;
  if (__builtin_expect(w8 < 4u, 1)) {
    const srd_t sWq = __builtin_amdgcn_make_buffer_rsrc((void*)(pwq + mem * 512u), 0, 448 * 8192 - mem * 512u, 0x00020000);
    const unsigned hi = l >> 5, row = (l >> 2) & 7u, col = hi * 64u + (l & 3u) * 16u;
    const unsigned wb = ldsb + w * 4096u;
    const unsigned vWq = (8u * w + row) * 8192u + col;
    unsigned sw = rot << 19;
    auto WA = [&](unsigned slot) { dma_cols<0, false>(wb + slot * 16384u, vWq, sWq, sw); };
    auto WB = [&](unsigned slot) { dma_cols<1, false>(wb + slot * 16384u, vWq, sWq, sw); sw += 0x40000u; sw = sw == 0x380000u ? 0u : sw; };
    WA(0); WB(0); WA(1); WB(1);
    PIN();
    u64 epoch;
    asm volatile("s_load_dwordx2 %0, %1, %2" : "=s"(epoch) : "s"(ws), "s"(grp * 256u) : "memory");
    {
      const srd_t sX = __builtin_amdgcn_make_buffer_rsrc((void*)(px + b * (64u * 896u)), 0, 64 * 896, 0x00020000);
      const unsigned xr = l >> 3;
      const unsigned v0 = xr * 896u + (((l & 7u) ^ (xr >> 1)) * 16u);
      unsigned pc = (j & 3u) * 14u + w;
      #pragma nounroll
      for (int i = 0; i < 14; ++i) {
        const unsigned rg = (pc * 37u) >> 8;
        dma_one(ldsb + L_XST + pc * 1024u, v0 ^ ((rg & 1u) << 6), sX, rg * (7168u - 896u) + pc * 128u);
        pc += 4u; pc = pc >= 56u ? pc - 56u : pc;
      }
    }
    WA(2); WB(2); WA(3); WB(3);
    const size_t kvoff = (size_t)(b * 8u + kvh) * (1024u * 512u);
    const srd_t sK  = __builtin_amdgcn_make_buffer_rsrc((void*)(pk + kvoff), 0, 524288, 0x00020000);
    const srd_t sV  = __builtin_amdgcn_make_buffer_rsrc((void*)(pv + kvoff), 0, 524288, 0x00020000);
    const srd_t sWo = __builtin_amdgcn_make_buffer_rsrc((void*)(pwo + mem * (256u * 896u)), 0, 256 * 896, 0x00020000);
    unsigned vK[4];
    { const unsigned kin = hi * 512u + (((l & 31u) ^ hi) * 16u);
      #pragma unroll
      for (int i = 0; i < 4; ++i) vK[i] = KPAT == 1 ? w * 4096u + l * 16u : w * 4096u + (kin ^ (((4u * w + i) & 7u) * 32u)); }
    const unsigned vV = (8u * w + row) * 512u + col;
    const unsigned vWo = (32u * w + row) * 896u + col;
    const unsigned krot = ROTK == 1 ? (b & 3u) * 8u : ROTK == 2 ? (kvh & 3u) * 8u : 0u;
    auto tile_of = [&](unsigned p, bool& nt) -> unsigned {
      if constexpr (SWP == 0) { nt = LOCK_NT; return (p + krot) & 31u; }
      else {
        const bool sw_ = (((p / (2u * SWP)) + b) % MIXPER) < MIXN;
        nt = sw_ ? ((p & SWP) != 0u) : true;
        return ((sw_ ? (p ^ (hs ? SWP : 0u)) : p) + krot) & 31u;
      }
    };
    auto KH = [&](auto hh, unsigned p, unsigned n) { if (NOKV == 1) return; if (NOKV == 2) p = 0; constexpr int H = decltype(hh)::value; bool nt; const unsigned t = tile_of(p, nt); const unsigned m = wb + ((n + 2u) & 7u) * 16384u;
      if (nt) dma_k<H, true>(m, vK, sK, t * 16384u); else dma_k<H, false>(m, vK, sK, t * 16384u); };
    auto VH = [&](auto hh, unsigned p, unsigned n) { if (NOKV == 1) return; if (NOKV == 2) p = 0; constexpr int H = decltype(hh)::value; bool nt; const unsigned t = tile_of(p, nt); const unsigned m = wb + ((n + 2u) & 7u) * 16384u;
      if (nt) dma_cols<H, true>(m, vV, sV, t * 16384u); else dma_cols<H, false>(m, vV, sV, t * 16384u); };
    unsigned co = rot * 128u;
    auto OA = [&](unsigned i) { if (!NOWO || i < 3) dma_wo<0>(wb + wo_slot(i) * 16384u, vWo, sWo, co + (i & 1u) * (128u * 896u)); };
    auto OB = [&](unsigned i) { if (!NOWO || i < 3) dma_wo<1>(wb + wo_slot(i) * 16384u, vWo, sWo, co + (i & 1u) * (128u * 896u)); if (i & 1u) { co += 128u; co = co == 896u ? 0u : co; } };
    unsigned tdummy = 0;
    auto touch = [&]() { const unsigned ta = l < 56u ? b * 28672u + w * 7168u + l * 128u : 0x7ffffff0u;
      asm volatile("buffer_load_dword %0, %1, %2, 0 offen" : "+v"(tdummy) : "v"(ta), "s"(sWo) : "memory"); };
    constexpr std::integral_constant<int, 0> A{}; constexpr std::integral_constant<int, 1> B{};
    asm volatile("s_waitcnt lgkmcnt(0)" : "+s"(epoch) :: "memory");
    vmwait<8>(); lbar();
    WA(4); vmwait<DBH>(); WB(4);
    unsigned sl = 5;
    #pragma nounroll
    for (int s = 1; s < 5; ++s) {
      vmwait<8>(); WA(sl); vmwait<DBH>(); WB(sl); sl = sl == 5u ? 0u : sl + 1u;
      vmwait<8>(); lbar();
      WA(sl); vmwait<DBH>(); WB(sl); sl = sl == 5u ? 0u : sl + 1u;
    }
    vmwait<8>(); WA(sl); vmwait<DBH>(); WB(sl);
    vmwait<8>(); lbar();
    KH(A, 0, 0); vmwait<DB>(); KH(B, 0, 0);
    vmwait<8>(); KH(A, 1, 1); vmwait<DB>(); KH(B, 1, 1);
    vmwait<8>(); lbar();
    KH(A, 2, 2); if (WOT == 3) touch();
    lbar();
    vmwait<DB>(); KH(B, 2, 2);
    vmwait<8>(); KH(A, 3, 3); vmwait<DB>(); KH(B, 3, 3);
    vmwait<8>(); KH(A, 4, 4); if (WOT == 1) touch(); lbar();
    vmwait<DB>(); KH(B, 4, 4);
    vmwait<8>(); VH(A, 0, 5); if (WOT == 2) touch(); lbar();
    vmwait<DB>(); VH(B, 0, 5);
    vmwait<8>(); KH(A, 5, 6); lbar();
    vmwait<DB>(); KH(B, 5, 6);
    constexpr unsigned P0 = 3;
    unsigned sdummy = 0;
    #pragma nounroll
    for (unsigned p = P0; p < 29u; ++p) {
      vmwait<8>(); VH(A, p - 2u, 2u * p + 1u); vmwait<DB>(); VH(B, p - 2u, 2u * p + 1u);
      vmwait<8>(); KH(A, p + 3u, 2u * p + 2u); lbar();
      vmwait<DB>(); KH(B, p + 3u, 2u * p + 2u);
    }
    asm volatile("s_waitcnt lgkmcnt(0)" : "+s"(sdummy), "+v"(tdummy) :: "memory");
    #pragma nounroll
    for (unsigned p = 29; p < 31u; ++p) {
      vmwait<8>(); VH(A, 2u * p - 31u, 2u * p + 1u); vmwait<DB>(); VH(B, 2u * p - 31u, 2u * p + 1u);
      vmwait<8>(); VH(A, 2u * p - 30u, 2u * p + 2u); lbar();
      vmwait<DB>(); VH(B, 2u * p - 30u, 2u * p + 2u);
    }
    vmwait<8>(); VH(A, 31, 63); lbar(); vmwait<DB>(); VH(B, 31, 63);
    vmwait<8>(); OA(0); lbar(); vmwait<DBH>(); OB(0);
    vmwait<8>(); OA(1); lbar(); vmwait<DBH>(); OB(1);
    vmwait<8>(); OA(2); lbar();
    vmwait<DBH>(); OB(2);
    vmwait<8>(); OA(3); vmwait<DBH>(); OB(3);
    #pragma unroll
    for (unsigned c = 0; c < 5u; ++c) {
      vmwait<8>(); OA(2u * c + 4u); if (!COFF) lbar(); vmwait<DBH>(); OB(2u * c + 4u);
      vmwait<8>(); OA(2u * c + 5u); if (COFF) lbar(); vmwait<DBH>(); OB(2u * c + 5u);
    }
    vmwait<8>(); lbar();
    vmwait<4>(); lbar();
    vmwait<0>(); lbar();
    tag = epoch + 0x9E3779B97F4A7C15ull;
  } else {
  CST(0);
  u64 epoch;
  asm volatile("s_load_dwordx2 %0, %1, %2\n\ts_waitcnt lgkmcnt(0)" : "=s"(epoch) : "s"(ws), "s"(grp * 256u) : "memory");
  tag = epoch + 0x9E3779B97F4A7C15ull;
  {
    const u64 pc = pc0;
    unsigned d = 0, o = ((j & 3u) * 4u + w) * 64u;
    unsigned len; asm volatile("s_mov_b32 %0, .Lgqa_end-.Lgqa_start" : "=s"(len));
    #pragma nounroll
    for (; o < len; o += 1024u) asm volatile("s_load_dword %0, %1, %2" : "+s"(d) : "s"(pc), "s"(o) : "memory");
    asm volatile("s_waitcnt lgkmcnt(0)" : "+s"(d) :: "memory");
  }
  const unsigned q16 = l & 15u, g = l >> 4, q32 = l & 31u, h = l >> 5, tp = l & 3u, tr = (l >> 2) & 3u;
  const unsigned perm = 8u * (4u * (tp & 1u) + 2u * (g & 1u) + (tp >> 1));
  f32x16 zero;
  #pragma unroll
  for (int e = 0; e < 16; ++e) zero[e] = 0.f;

  bf16x8 qf[8];
  {
    f32x16 acc[2][2];
    const unsigned wqb = ldsb + h * 4096u + w * 1024u + tr * 64u + perm;
    const unsigned wq2 = opq(wqb + 32768u);
    const unsigned xsw = (q32 >> 1) & 7u;
    const unsigned xb = ldsb + L_XST + (q32 >> 3) * 7168u + (q32 & 7u) * 128u;
    unsigned xo[4];
    #pragma unroll
    for (int t = 0; t < 4; ++t) xo[t] = xb + (((2u * t + h) ^ xsw) * 16u);
    const unsigned qw = ldsb + L_EXT + q32 * 512u;
    unsigned kb = rot;
    bf16x8 xf[2][4][2], wf[2][4][2];
    #pragma unroll
    for (int s = 0; s < 7; ++s) {
      cbar();
      if (s == 0) CST(1);
      const unsigned a = wqb + ((2 * s) % 6) * 16384u, xk = kb * 1024u;
      #pragma unroll
      for (int u = 0; u < (s < 6 ? 4 : 2); ++u) {
        #pragma unroll
        for (int z = 0; z < 4; ++z) {
          if (u == 0) { if (s > 0) acc[z >> 1][z & 1] = mfma32(wf[(s - 1) & 1][3][z >> 1], xf[(s - 1) & 1][3][z & 1], acc[z >> 1][z & 1]); }
          else acc[z >> 1][z & 1] = mfma32(wf[s & 1][u - 1][z >> 1], xf[s & 1][u - 1][z & 1], (s == 0 && u == 1) ? zero : acc[z >> 1][z & 1]);
          PIN();
          if (u < 2) {
            #pragma unroll
            for (int f = 8 * u + 2 * z; f < 8 * u + 2 * z + 2; ++f) {
              const int tt = f >> 2, k = f & 3;
              if (k == 0 || k == 3) { const unsigned aa = (((2 * s) % 6) >= 2 ? wq2 + (((2 * s) % 6) - 2) * 16384u : a) + tt * 8192u + (k & 1) * 512u; wf[s & 1][tt][k & 1] = trfrag(aa, aa + 256u); }
              else xf[s & 1][tt][k - 1] = ldsfrag(xo[tt] + xk + (k - 1) * (4u * 7168u));
            }
            PIN();
          }
        }
      }
      kb = kb == 6u ? 0u : kb + 1u;
    }
    #pragma unroll
    for (int z = 0; z < 5; ++z) {
      #pragma unroll
      for (int t = 1; t < 4; ++t) {
        if (z < 4) acc[z >> 1][z & 1] = mfma32(wf[0][t][z >> 1], xf[0][t][z & 1], acc[z >> 1][z & 1]);
        PIN();
        if (z > 0 && t >= 2) {
          const int y = z - 1, i = t - 2;
          const f32x16& a = acc[y >> 1][y & 1];
          u32x4 v = {cvtpk(a[8 * i], a[8 * i + 1]), cvtpk(a[8 * i + 2], a[8 * i + 3]), cvtpk(a[8 * i + 4], a[8 * i + 5]), cvtpk(a[8 * i + 6], a[8 * i + 7])};
          *(LAS u32x4*)(size_t)(qw + (y & 1) * 16384u + (((8u * w + 4u * (y >> 1) + 2u * h + i) ^ q16) * 16u)) = v;
          PIN();
        }
      }
      if (z == 0) { cbar(); CST(2); }
    }
    CST(3);
  }
  STAMP_AT(1);
  f32x16 O[2][2];
  #pragma unroll
  for (int i = 0; i < 4; ++i) O[i >> 1][i & 1] = zero;
  unsigned kb[4];
  #pragma unroll
  for (int u = 0; u < 4; ++u) kb[u] = ldsb + q16 * 512u + (((4u * u + g) ^ q16) * 16u);
  const unsigned vb = ldsb + w * 1024u + (4u * h + tr) * 64u + 32u * (g & 1u) + 8u * tp;
  const unsigned pw = ldsb + L_EXT + w * 8192u + l * 16u;
  const unsigned pr = ldsb + L_EXT + (g & 1u) * 8192u + (h * 16u + q16) * 16u;
  const unsigned aw = ldsb + L_EXT + w * 8192u + L_AL + q16 * 4u, ar = ldsb + L_EXT + (g & 1u) * 8192u + L_AL + q16 * 4u;
  const float cs = 0.0625f * 1.4426950408889634f;
  float mref = 0.f, lsum = 0.f;

  struct PVF { bf16x8 pf[2][2], vf[2][2]; float a0, a1; };
  auto pv_read = [&](PVF& f, int i, unsigned va, unsigned pa) {
    const int ks = i >> 2, k = i & 3;
    if (k == 0) f.vf[0][ks] = trfrag(va + ks * 4096u, va + ks * 4096u + 8192u);
    if (k == 1) f.pf[0][ks] = ldsfrag(pa + ks * 512u);
    if (k == 2) f.pf[1][ks] = ldsfrag(pa + 16384u + ks * 512u);
    if (k == 3) f.vf[1][ks] = trfrag(va + ks * 4096u + 512u, va + ks * 4096u + 512u + 8192u);
  };
  auto pv_m = [&](const PVF& f, int i) { const int ks = i >> 2, db = (i >> 1) & 1, qb = i & 1; O[db][qb] = mfma32(f.vf[db][ks], f.pf[qb][ks], O[db][qb]); };
  auto rescale = [&](float a0, float a1) {
    if (__builtin_expect(__any(a0 != 1.f || a1 != 1.f), 0)) {
      #pragma unroll
      for (int db = 0; db < 2; ++db)
        #pragma unroll
        for (int e = 0; e < 16; ++e) { O[db][0][e] *= a0; O[db][1][e] *= a1; }
    }
  };
  bf16x8 kf[16];
  auto k_read = [&](int i, const unsigned (&kk)[4]) { kf[i] = ldsfrag(kk[(i >> 1) & 3] + (i >> 3) * 256u + (i & 1) * 8192u); };
  auto phase = [&](auto mode, unsigned kslot, unsigned vslot, unsigned p) {
    constexpr int MODE = decltype(mode)::value;
    constexpr bool FIRST = MODE == 0, PV = MODE >= 2, LAST = MODE == 3;
    PVF f;
    unsigned kk[4], va = 0, pa = 0;
    if constexpr (PV) {
      const unsigned rr = (p + 1u) & 3u;
      va = opq(vb + vslot); pa = opq(pr + rr * 1024u);
      const unsigned aa = ar + rr * 64u; f.a0 = ldsf(aa); f.a1 = ldsf(aa + 16384u);
    }
    PIN();
    f32x4 s0, s1;
    #pragma unroll
    for (int i = 0; i < 16; ++i) {
      if (i == 0) s0 = mfma16(kf[0], qf[0], f32x4{0.f, 0.f, 0.f, 0.f});
      else if (i == 1) s1 = mfma16(kf[1], qf[0], f32x4{0.f, 0.f, 0.f, 0.f});
      else if (i & 1) s1 = mfma16(kf[i], qf[i >> 1], s1);
      else s0 = mfma16(kf[i], qf[i >> 1], s0);
      if constexpr (PV) {
        if (i < 8) pv_read(f, i, va, pa);
        if (!LAST && i >= 4 && i < 8) kk[i - 4] = opq(kb[i - 4] + kslot);
        if (!LAST && i >= 8) k_read(i - 8, kk);
      } else {
        if (i < 8 && (i & 1) == 0) kk[i >> 1] = opq(kb[i >> 1] + kslot);
        k_read(i, kk);
      }
      PIN();
    }
    constexpr bool KR = PV && !LAST;
    asm volatile("" ::: "memory");
    if constexpr (PV) { rescale(f.a0, f.a1); PIN(); pv_m(f, 0); }
    if constexpr (KR) k_read(8, kk);
    PIN();
    float t[8];
    #pragma unroll
    for (int e = 0; e < 4; ++e) t[e] = __builtin_fmaf(s0[e], cs, -mref);
    PIN();
    if constexpr (PV) pv_m(f, 1);
    if constexpr (KR) k_read(9, kk);
    PIN();
    #pragma unroll
    for (int e = 0; e < 4; ++e) t[4 + e] = __builtin_fmaf(s1[e], cs, -mref);
    float mx = fmaxf(fmaxf(t[0], t[1]), fmaxf(t[2], t[3]));
    PIN();
    if constexpr (PV) pv_m(f, 2);
    if constexpr (KR) k_read(10, kk);
    asm volatile("" ::: "memory");
    PIN();
    mx = fmaxf(fmaxf(fmaxf(mx, t[4]), t[5]), fmaxf(t[6], t[7]));
    float alpha = 1.f;
    if (FIRST || __builtin_expect(__any(mx > THR), 0)) {
      auto r = __builtin_amdgcn_permlane16_swap(__float_as_uint(mx), __float_as_uint(mx), false, false);
      mx = fmaxf(__uint_as_float(r[0]), __uint_as_float(r[1]));
      r = __builtin_amdgcn_permlane32_swap(__float_as_uint(mx), __float_as_uint(mx), false, false);
      mx = fmaxf(__uint_as_float(r[0]), __uint_as_float(r[1]));
      if constexpr (!FIRST) { mx = fmaxf(mx, 0.f); alpha = __builtin_amdgcn_exp2f(-mx); lsum *= alpha; }
      mref += mx;
      #pragma unroll
      for (int e = 0; e < 8; ++e) t[e] -= mx;
    }
    PIN();
    #pragma unroll
    for (int z = 0; z < 3; ++z) {
      if constexpr (PV) pv_m(f, 3 + z);
      if constexpr (KR) { k_read(11 + z, kk); if (z == 2) k_read(14, kk); }
      PIN();
      #pragma unroll
      for (int e = 3 * z; e < 3 * z + 3 && e < 8; ++e) { t[e] = __builtin_amdgcn_exp2f(t[e]); asm volatile("" : "+v"(t[e])); }
      PIN();
    }
    if constexpr (PV && !LAST) { pv_m(f, 6); k_read(15, kk); PIN(); }
    if constexpr (LAST) {
      lsum += ((t[0] + t[1]) + (t[2] + t[3])) + ((t[4] + t[5]) + (t[6] + t[7]));
      auto r = __builtin_amdgcn_permlane16_swap(__float_as_uint(lsum), __float_as_uint(lsum), false, false);
      lsum = __uint_as_float(r[0]) + __uint_as_float(r[1]);
      r = __builtin_amdgcn_permlane32_swap(__float_as_uint(lsum), __float_as_uint(lsum), false, false);
      const float li = __builtin_amdgcn_rcpf(__uint_as_float(r[0]) + __uint_as_float(r[1]));
      PIN(); pv_m(f, 6); PIN();
      alpha *= li;
      #pragma unroll
      for (int e = 0; e < 8; ++e) t[e] *= li;
    }
    u32x4 v = {cvtpk(t[0], t[1]), cvtpk(t[2], t[3]), cvtpk(t[4], t[5]), cvtpk(t[6], t[7])};
    *(LAS u32x4*)(size_t)(pw + (p & 3u) * 1024u) = v;
    *(LAS float*)(size_t)(aw + (p & 3u) * 64u) = alpha;
    PIN();
    if constexpr (PV) { pv_m(f, 7); PIN(); }
    if constexpr (!LAST) lsum += ((t[0] + t[1]) + (t[2] + t[3])) + ((t[4] + t[5]) + (t[6] + t[7]));
    PIN();
  };

  CST(4);
  cbar();
  CST(5);
  {
    unsigned kk[4];
    #pragma unroll
    for (int u = 0; u < 4; ++u) kk[u] = opq(kb[u] + 2u * 16384u);
    #pragma unroll
    for (int u = 0; u < 8; ++u) { qf[u] = ldsfrag(kb[u & 3] + L_EXT + w * 8192u + (u >> 2) * 256u); k_read(2 * u, kk); k_read(2 * u + 1, kk); PIN(); }
  }
  CST(6);
  phase(std::integral_constant<int, 0>{}, 3u * 16384u, 0u, 0u);
  CST(7);
  #pragma nounroll
  for (unsigned p = 1; p < 3u; ++p) { cbar(); phase(std::integral_constant<int, 1>{}, (p + 3u) * 16384u, 0u, p); }
  #pragma nounroll
  for (unsigned p = 3; p < 31u; ++p) {
    if (!NOBAR || p >= 29u) cbar();
    phase(std::integral_constant<int, 2>{}, ((2u * p) & 7u) * 16384u, ((2u * p + 1u) & 7u) * 16384u, p);
  }
  cbar();
  CST(8);
  phase(std::integral_constant<int, 3>{}, 0u, 6u * 16384u, 31u);
  const unsigned ost = ldsb + L_EXT + l * 16u;
  const unsigned wob = ldsb + (w & 1u) * 512u + (4u * h + tr) * 64u + perm;
  const unsigned pst = WS_PART + (grp * 16u + mem) * (7u * 8192u) + w * 2048u + l * 16u;
  const unsigned fl = l == 0 ? WS_FLAG + ((grp * 4u + w) * 16u + mem) * FS : 0x7ffffff0u;
  {
    PVF f0, f1, f;
    auto pre = [&](PVF& d, unsigned rr) {
      d.a0 = ldsf(ar + rr * 64u); d.a1 = ldsf(ar + rr * 64u + 16384u);
      #pragma unroll
      for (int i = 0; i < 8; ++i) if ((i & 3) == 1 || (i & 3) == 2) pv_read(d, i, 0u, pr + rr * 1024u);
      PIN();
    };
    auto vrd = [&](PVF& d, unsigned va) {
      #pragma unroll
      for (int i = 0; i < 4; ++i) { pv_read(d, (i >> 1) * 4 + (i & 1) * 3, va, 0u); PIN(); }
    };
    const unsigned v7 = opq(vb + 7u * 16384u), ow = opq(ost + w * 4096u);
    pre(f0, 1u); pre(f1, 2u);
    asm volatile("" :: "v"(wob), "v"(pst), "v"(fl));
    rescale(f0.a0, f0.a1);
    cbar();
    vrd(f0, v7); pre(f, 3u);
    #pragma unroll
    for (int i = 0; i < 8; ++i) { pv_m(f0, i); PIN(); }
    rescale(f1.a0, f1.a1);
    cbar();
    vrd(f1, vb);
    #pragma unroll
    for (int i = 0; i < 8; ++i) { pv_m(f1, (i >> 1) + 4 * (i & 1)); PIN();
      if (i >= 3 && (i & 1)) { const int z = (i - 3) >> 1; O[z >> 1][z & 1] *= (z & 1) ? f.a1 : f.a0; asm volatile("" : "+v"(O[z >> 1][z & 1])); PIN(); } }
    O[1][1] *= f.a1; asm volatile("" : "+v"(O[1][1]));
    CST(9);
    STAMP_AT(2);
    cbar();
    CST(10);
    STAMP_AT(3);
    #pragma unroll
    for (int i = 0; i < 4; ++i) { pv_read(f, (i & 1) * 4 + (i >> 1) * 3, vb + 16384u, 0u); PIN(); }
    pv_m(f, 0); pv_m(f, 4); PIN();
    CST(11);
    #pragma unroll
    for (int z = 0; z < 4; ++z) {
      const f32x16& a = O[z >> 1][z & 1];
      #pragma unroll
      for (int i = 0; i < 2; ++i) {
        if (z < 3) { pv_m(f, z + 1 + 4 * i); PIN(); }
        u32x4 v = {cvtpk(a[8 * i], a[8 * i + 1]), cvtpk(a[8 * i + 2], a[8 * i + 3]), cvtpk(a[8 * i + 4], a[8 * i + 5]), cvtpk(a[8 * i + 6], a[8 * i + 7])};
        *(LAS u32x4*)(size_t)(ow + (z & 1) * 16384u + (2u * (z >> 1) + i) * 1024u) = v;
        PIN();
      }
    }
  }
  CST(12);
  STAMP_AT(4);
  {
    bf16x8 of[16];
    const u32x2 tg = {(unsigned)tag, (unsigned)(tag >> 32)};
    unsigned ch = rot;
    bf16x8 fr[8];
    f32x16 acc[2];
    u32x4 res[2];
    auto pack = [&](int c, int k) {
      const f32x16& a = acc[c & 1];
      res[k >> 2][k & 3] = cvtpk(a[2 * k], a[2 * k + 1]);
      asm volatile("" : "+v"(res[k >> 2]));
    };
    auto put = [&](int i) { if (NOST) asm volatile("" :: "v"(res[i])); else __builtin_amdgcn_raw_buffer_store_b128(res[i], sWs, pst + i * 1024u, ch * 8192u, 16); if (i) ch = ch == 6u ? 0u : ch + 1u; };
    const unsigned ord = ost + (w >> 1) * 16384u;
    unsigned alast = 0;
    #pragma unroll
    for (int c = 0; c < 8; ++c) {
      unsigned a = 0;
      if (c < 7) {
        if (c <= 2) cbar(); else { PIN(); asm volatile("s_barrier" ::: "memory"); PIN(); }
        if (c == 0) CST(13);
        if (c == 6) CST(14);
        a = opq(wob + wo_slot(2 * c) * 16384u);
      }
      if (c == 7) {
        PIN(); asm volatile("s_barrier" ::: "memory"); PIN();
        #pragma unroll
        for (int s = 8; s < 16; ++s) { fr[s & 7] = trfrag(alast + s * 2048u, alast + s * 2048u + 1024u); PIN(); }
      }
      alast = a;
      #pragma unroll
      for (int s = 0; s < 16; ++s) {
        if (s < 8) { if (c > 0) acc[(c - 1) & 1] = mfma32(fr[s], of[8 + s], acc[(c - 1) & 1]); }
        else if (c < 7) acc[c & 1] = mfma32(fr[s & 7], of[s - 8], s == 8 ? zero : acc[c & 1]);
        PIN();
        if (c == 0) of[s] = ldsfrag(ord + s * 1024u);
        if (c < 6 || (c == 6 && s < 8)) fr[s & 7] = trfrag(a + s * 2048u, a + s * 2048u + 1024u);
        PIN();
        if (c > 0 && (s == 10 || s == 11 || s == 13 || s == 14)) { const int k = s < 12 ? 2 * (s - 10) : 2 * (s - 11); pack(c - 1, k); pack(c - 1, k + 1); PIN(); }
        if (c > 0 && (s == 12 || s == 15)) { put(s == 15); PIN(); }
        if (EARLY && c == 7 && s == 1) { vmwait<2 * (6 - EARLY)>(); __builtin_amdgcn_raw_buffer_store_b64(tg, sWs, l == 0 ? fl + 1024u * FS : 0x7ffffff0u, 0, 16); PIN(); }
      }
    }
    CST(15);
    vmwait<0>();
    CST(16);
    __builtin_amdgcn_raw_buffer_store_b64(tg, sWs, fl, 0, 16);
  }
  asm volatile(".Lgqa_end:");
  STAMP_AT(5);
  }
  if (POLL || w8 < 7u) {
    unsigned rc = w8 + rot; rc = w8 == 7u ? 7u : rc >= 7u ? rc - 7u : rc;
    reduce_chunk(sWs, out, grp, mem, rc, l, b, br, tag, (EARLY && w8 < EARLY) ? 1u : 0u, w8 == PWAVE);
    if (w8 == 0u && mem == 0u) {
      u32x2 t = {(unsigned)tag, (unsigned)(tag >> 32)};
      __builtin_amdgcn_raw_buffer_store_b64(t, sWs, l == 0 ? WS_EPOCH + grp * 256u : 0x7ffffff0u, 0, 16);
    }
  }
  STAMP_AT(6);
}

extern "C" void kernel_launch(void* const* d_in, const int* in_sizes, int n_in, void* d_out, int out_size, void* d_ws, size_t ws_size, hipStream_t stream) {
  (void)in_sizes; (void)n_in; (void)out_size; (void)ws_size;
  (void)hipFuncSetAttribute((const void*)gqa2_fused, hipFuncAttributeMaxDynamicSharedMemorySize, 163840);
  Args a;
  for (int i = 0; i < 10; ++i) a.in[i] = (const char*)d_in[i];
  a.out = (char*)d_out; a.ws = (char*)d_ws;
  hipLaunchKernelGGL(gqa2_fused, dim3(256), dim3(512), 163840, stream, a);
}
```

```cpp
#define STAMP 1
#include <hip/hip_runtime.h>
#include <type_traits>

typedef __bf16   bf16x8 __attribute__((ext_vector_type(8)));
typedef __bf16   bf16x4 __attribute__((ext_vector_type(4)));
typedef __bf16   bf16x2 __attribute__((ext_vector_type(2)));
typedef float    f32x4  __attribute__((ext_vector_type(4)));
typedef float    f32x16 __attribute__((ext_vector_type(16)));
typedef float    f32x2  __attribute__((ext_vector_type(2)));
typedef unsigned u32x4  __attribute__((ext_vector_type(4)));
typedef unsigned u32x2  __attribute__((ext_vector_type(2)));
typedef unsigned long long u64;
typedef __amdgpu_buffer_rsrc_t srd_t;
#define LAS __attribute__((address_space(3)))
#define DI __device__ __forceinline__
#define PIN() __builtin_amdgcn_sched_barrier(0)

#define SWP 0
#define MIXPER 1
#define MIXN 1
#define LOCK_NT 1
#define CODEPF 1
#define ROTK 1
#define WOPF 0
#define PRETOUCH 0
#define NOWO 0
#define NOST 0
#define LAUX 16
#define COFF 1
#define DEEP 0
#define NOCOMP 0
#define KPAT 0
#define NOBAR 0
#define RUNA 0
#define RUNE 0
#define WOT 0
#define NOKV 0
#define BAL 1
#define RED 0
#define EARLY 0
#define NORED 0
#define POLL 1
#define PGAP 2
#define PMODE 0
#define PWAVE 7
#define HEAD2 1
#define DBH 8
#define DB 8

constexpr unsigned WS_EPOCH = 0;
#define FS 8
constexpr unsigned WS_FLAG  = 4096;
constexpr unsigned WS_PART  = 1u << 20;
constexpr unsigned WS_PART_BYTES = 256u * 7u * 8192u;
constexpr unsigned WS_END   = WS_PART + WS_PART_BYTES;
constexpr unsigned WS_STAMP = 32u << 20;
constexpr int NSTAMP = 7, NBAR = 51, LW0 = 0;
constexpr unsigned L_XST = 163840u - 57344u;
constexpr unsigned L_EXT = 131072u;
constexpr unsigned L_AL  = 4096u;
constexpr float THR = 8.0f;
constexpr unsigned wo_slot(unsigned i) { return i < 8u ? (i + 2u) & 7u : i < 10u ? i : i - 8u; }

struct Args { const char* in[10]; char* out; char* ws; };

DI unsigned cvtpk(float lo, float hi) { f32x2 v = {lo, hi}; bf16x2 b = __builtin_convertvector(v, bf16x2); return __builtin_bit_cast(unsigned, b); }
DI f32x4 mfma16(bf16x8 a, bf16x8 b, f32x4 c) { return __builtin_amdgcn_mfma_f32_16x16x32_bf16(a, b, c, 0, 0, 0); }
DI f32x16 mfma32(bf16x8 a, bf16x8 b, f32x16 c) { return __builtin_amdgcn_mfma_f32_32x32x16_bf16(a, b, c, 0, 0, 0); }
DI bf16x8 trfrag(unsigned a0, unsigned a1) {
  bf16x4 lo = __builtin_amdgcn_ds_read_tr16_b64_v4bf16((LAS bf16x4*)(size_t)a0);
  bf16x4 hi = __builtin_amdgcn_ds_read_tr16_b64_v4bf16((LAS bf16x4*)(size_t)a1);
  return __builtin_shufflevector(lo, hi, 0, 1, 2, 3, 4, 5, 6, 7);
}
DI unsigned opq(unsigned a) { asm volatile("" : "+v"(a)); return a; }
DI bf16x8 ldsfrag(unsigned a) { return *(const LAS bf16x8*)(size_t)a; }
DI float ldsf(unsigned a) { return *(const LAS float*)(size_t)a; }
DI void cbar() { PIN(); asm volatile("s_waitcnt lgkmcnt(0)\n\ts_barrier" ::: "memory"); PIN(); }
DI void lbar() { asm volatile("s_barrier" ::: "memory"); }
template <int N> DI void vmwait() { asm volatile("s_waitcnt vmcnt(%0)" :: "i"(N) : "memory"); }

template <int H, bool NT> DI void dma_cols(unsigned m, unsigned v, srd_t srd, unsigned so) {
  if constexpr (NT)
    asm volatile("s_add_u32 m0, %0, %4\n\ts_nop 0\n\tbuffer_load_dwordx4 %1, %2, %3 offen offset:%5 nt lds\n\t"
                 "s_add_u32 m0, %0, %6\n\ts_nop 0\n\tbuffer_load_dwordx4 %1, %2, %3 offen offset:%7 nt lds"
                 :: "s"(m), "v"(v), "s"(srd), "s"(so), "i"(H * 1792), "i"(H * 256), "i"(H * 1792 + 896), "i"(H * 256 + 128) : "memory", "scc");
  else
    asm volatile("s_add_u32 m0, %0, %4\n\ts_nop 0\n\tbuffer_load_dwordx4 %1, %2, %3 offen offset:%5 lds\n\t"
                 "s_add_u32 m0, %0, %6\n\ts_nop 0\n\tbuffer_load_dwordx4 %1, %2, %3 offen offset:%7 lds"
                 :: "s"(m), "v"(v), "s"(srd), "s"(so), "i"(H * 1792), "i"(H * 256), "i"(H * 1792 + 896), "i"(H * 256 + 128) : "memory", "scc");
}
template <int H, bool NT> DI void dma_k(unsigned m, const unsigned (&v)[4], srd_t srd, unsigned so) {
  if constexpr (NT)
    asm volatile("s_mov_b32 m0, %0\n\ts_nop 0\n\tbuffer_load_dwordx4 %1, %3, %4 offen offset:%5 nt lds\n\tbuffer_load_dwordx4 %2, %3, %4 offen offset:%6 nt lds"
                 :: "s"(m), "v"(v[2 * H]), "v"(v[2 * H + 1]), "s"(srd), "s"(so), "i"(H * 2048), "i"(H * 2048 + 1024) : "memory");
  else
    asm volatile("s_mov_b32 m0, %0\n\ts_nop 0\n\tbuffer_load_dwordx4 %1, %3, %4 offen offset:%5 lds\n\tbuffer_load_dwordx4 %2, %3, %4 offen offset:%6 lds"
                 :: "s"(m), "v"(v[2 * H]), "v"(v[2 * H + 1]), "s"(srd), "s"(so), "i"(H * 2048), "i"(H * 2048 + 1024) : "memory");
}
template <int H> DI void dma_wo(unsigned m, unsigned v, srd_t srd, unsigned so) {
  unsigned t;
  asm volatile("s_add_u32 m0, %1, %5\n\ts_add_u32 %0, %4, %6\n\tbuffer_load_dwordx4 %2, %3, %0 offen lds\n\t"
               "s_add_u32 m0, %1, %7\n\ts_add_u32 %0, %4, %8\n\tbuffer_load_dwordx4 %2, %3, %0 offen lds"
               : "=&s"(t) : "s"(m), "v"(v), "s"(srd), "s"(so), "i"(H * 2048), "i"(H * 14336), "i"(H * 2048 + 1024), "i"(H * 14336 + 7168) : "memory", "scc");
}
DI void dma_one(unsigned m, unsigned v, srd_t srd, unsigned so) {
  asm volatile("s_mov_b32 m0, %0\n\ts_nop 0\n\tbuffer_load_dwordx4 %1, %2, %3 offen lds" :: "s"(m), "v"(v), "s"(srd), "s"(so) : "memory");
}

#define STAMP_AT(i) do { if ((STAMP < 4 || i == 0 || i >= 5) && threadIdx.x == 256) { stamps[i] = __builtin_amdgcn_s_memrealtime(); stamps[8 + i] = __builtin_amdgcn_s_memtime(); } } while (0)
#define CST(i) do {} while (0)

#define PSLEEP 1
DI void reduce_chunk(srd_t sWs, char* out, unsigned grp, unsigned mem, unsigned ch, unsigned l, unsigned b, unsigned br, u64 tag, unsigned early, bool poller) {
  const unsigned fa = WS_FLAG + early * (1024u * FS) + ((grp * 4u + (mem >> 2)) * 16u + (l & 15u)) * FS;
  const unsigned pa = WS_PART + (grp * 16u + (l >> 5)) * (7u * 8192u) + ch * 8192u + mem * 512u + (l & 31u) * 16u;
  const unsigned wv = mem >> 2, q = 32u * (wv >> 1) + (l & 31u), n = 64u * ch + 32u * (wv & 1u) + 16u * (mem & 1u) + 8u * ((mem >> 1) & 1u) + 4u * (l >> 5);
  char* const po = out + (size_t)(b * 64u + q) * 1792u + br * 896u + n * 2u;
  asm volatile("" :: "v"(po), "v"(pa));
  u32x2 pend[POLL];
  #pragma unroll
  for (int i = 0; i < POLL; ++i) pend[i] = u32x2{0u, 0u};
  if (poller) {
    #pragma nounroll
    for (unsigned spins = 0; spins < (1u << 20); ++spins) {
      u32x2 fv = {0u, 0u};
      asm volatile("buffer_load_dwordx2 %0, %1, %2, 0 offen sc1\n\ts_waitcnt vmcnt(0)" : "+v"(fv) : "v"(fa), "s"(sWs) : "memory");
      if ((__ballot(fv[0] == (unsigned)tag && fv[1] == (unsigned)(tag >> 32)) & 0xffffu) == 0xffffu) break;
      if (PGAP) __builtin_amdgcn_s_sleep(PGAP);
    }
  }
  asm volatile("s_barrier" ::: "memory");
  if (ch >= 7u) return;
  #pragma nounroll
  for (unsigned spins = 0; spins < ((NORED == 2 || POLL) ? 0u : 1u << 20); ++spins) {
    u32x2 fv;
    asm volatile("buffer_load_dwordx2 %0, %1, %2, 0 offen sc1\n\ts_waitcnt vmcnt(0)" : "=v"(fv) : "v"(fa), "s"(sWs) : "memory");
    if (__all(fv[0] == (unsigned)tag && fv[1] == (unsigned)(tag >> 32))) break;
    if (PSLEEP) __builtin_amdgcn_s_sleep(PSLEEP);
  }
  u32x4 r[8];
  #pragma unroll
  for (int i = 0; i < 8; ++i) r[i] = __builtin_amdgcn_raw_buffer_load_b128(sWs, (NORED == 1 || NORED == 2 || (NORED == 3 && i >= 4) || (NORED == 4 && i >= 1)) ? 0x7ffffff0u : pa, i * (2u * 7u * 8192u), LAUX);
  float a[8];
  #pragma unroll
  for (int i = 0; i < 8; ++i) {
    #pragma unroll
    for (int e = 0; e < 4; ++e) {
      const float lo = __uint_as_float(r[i][e] << 16), hi = __uint_as_float(r[i][e] & 0xffff0000u);
      a[2 * e] = i ? a[2 * e] + lo : lo; a[2 * e + 1] = i ? a[2 * e + 1] + hi : hi;
    }
    PIN();
  }
  #pragma unroll
  for (int i = 0; i < POLL; ++i) asm volatile("" :: "v"(pend[i]));
  float o[4];
  #pragma unroll
  for (int e = 0; e < 4; ++e) {
    auto s = __builtin_amdgcn_permlane32_swap(__float_as_uint(a[e]), __float_as_uint(a[4 + e]), false, false);
    o[e] = __uint_as_float(s[0]) + __uint_as_float(s[1]);
  }
  *(u32x2*)po = u32x2{cvtpk(o[0], o[1]), cvtpk(o[2], o[3])};
}

__global__ __launch_bounds__(512, 2)
void gqa2_fused(Args args) {
  extern __shared__ __attribute__((aligned(16))) char lds[];
  u64 pc0; asm volatile("s_getpc_b64 %0\n.Lgqa_start:" : "=s"(pc0));
  const unsigned bid = blockIdx.x, br = (bid >> 2) & 1u, j = bid >> 3, hs = j & 1u, b = j >> 2;
  const char* const px = args.in[br * 5u], * const pk = args.in[br * 5u + 1u], * const pv = args.in[br * 5u + 2u], * const pwq = args.in[br * 5u + 3u], * const pwo = args.in[br * 5u + 4u];
  char* const out = args.out; char* const ws = args.ws;
  asm volatile("" :: "s"(px), "s"(pk), "s"(pv), "s"(pwq), "s"(pwo), "s"(out), "s"(ws));
  const unsigned kvh = (bid & 3u) * 2u + ((j >> 1) & 1u), grp = br * 8u + b, mem = kvh * 2u + hs;
  const unsigned tid = threadIdx.x, l = tid & 63u;
  const unsigned w8 = __builtin_amdgcn_readfirstlane(tid >> 6), w = w8 & 3u;
  const unsigned rot = b == 7u ? 3u : b;
  const unsigned ldsb = (unsigned)(size_t)lds;
  u64* stamps = (u64*)(ws + WS_STAMP) + bid * 16;
  STAMP_AT(0);
  const srd_t sWs = __builtin_amdgcn_make_buffer_rsrc((void*)ws, 0, WS_END, 0x00020000);

  u64 tag;
  if (__builtin_expect(w8 < 4u, 1)) {
    const srd_t sWq = __builtin_amdgcn_make_buffer_rsrc((void*)(pwq + mem * 512u), 0, 448 * 8192 - mem * 512u, 0x00020000);
    const unsigned hi = l >> 5, row = (l >> 2) & 7u, col = hi * 64u + (l & 3u) * 16u;
    const unsigned wb = ldsb + w * 4096u;
    const unsigned vWq = (8u * w + row) * 8192u + col;
    unsigned sw = rot << 19;
    auto WA = [&](unsigned slot) { dma_cols<0, false>(wb + slot * 16384u, vWq, sWq, sw); };
    auto WB = [&](unsigned slot) { dma_cols<1, false>(wb + slot * 16384u, vWq, sWq, sw); sw += 0x40000u; sw = sw == 0x380000u ? 0u : sw; };
    WA(0); WB(0); WA(1); WB(1);
    PIN();
    u64 epoch;
    asm volatile("s_load_dwordx2 %0, %1, %2" : "=s"(epoch) : "s"(ws), "s"(grp * 256u) : "memory");
    {
      const srd_t sX = __builtin_amdgcn_make_buffer_rsrc((void*)(px + b * (64u * 896u)), 0, 64 * 896, 0x00020000);
      const unsigned xr = l >> 3;
      const unsigned v0 = xr * 896u + (((l & 7u) ^ (xr >> 1)) * 16u);
      unsigned pc = (j & 3u) * 14u + w;
      #pragma nounroll
      for (int i = 0; i < 14; ++i) {
        const unsigned rg = (pc * 37u) >> 8;
        dma_one(ldsb + L_XST + pc * 1024u, v0 ^ ((rg & 1u) << 6), sX, rg * (7168u - 896u) + pc * 128u);
        pc += 4u; pc = pc >= 56u ? pc - 56u : pc;
      }
    }
    WA(2); WB(2); WA(3); WB(3);
    const size_t kvoff = (size_t)(b * 8u + kvh) * (1024u * 512u);
    const srd_t sK  = __builtin_amdgcn_make_buffer_rsrc((void*)(pk + kvoff), 0, 524288, 0x00020000);
    const srd_t sV  = __builtin_amdgcn_make_buffer_rsrc((void*)(pv + kvoff), 0, 524288, 0x00020000);
    const srd_t sWo = __builtin_amdgcn_make_buffer_rsrc((void*)(pwo + mem * (256u * 896u)), 0, 256 * 896, 0x00020000);
    unsigned vK[4];
    { const unsigned kin = hi * 512u + (((l & 31u) ^ hi) * 16u);
      #pragma unroll
      for (int i = 0; i < 4; ++i) vK[i] = KPAT == 1 ? w * 4096u + l * 16u : w * 4096u + (kin ^ (((4u * w + i) & 7u) * 32u)); }
    const unsigned vV = (8u * w + row) * 512u + col;
    const unsigned vWo = (32u * w + row) * 896u + col;
    const unsigned krot = ROTK == 1 ? (b & 3u) * 8u : ROTK == 2 ? (kvh & 3u) * 8u : 0u;
    auto tile_of = [&](unsigned p, bool& nt) -> unsigned {
      if constexpr (SWP == 0) { nt = LOCK_NT; return (p + krot) & 31u; }
      else {
        const bool sw_ = (((p / (2u * SWP)) + b) % MIXPER) < MIXN;
        nt = sw_ ? ((p & SWP) != 0u) : true;
        return ((sw_ ? (p ^ (hs ? SWP : 0u)) : p) + krot) & 31u;
      }
    };
    auto KH = [&](auto hh, unsigned p, unsigned n) { if (NOKV == 1) return; if (NOKV == 2) p = 0; constexpr int H = decltype(hh)::value; bool nt; const unsigned t = tile_of(p, nt); const unsigned m = wb + ((n + 2u) & 7u) * 16384u;
      if (nt) dma_k<H, true>(m, vK, sK, t * 16384u); else dma_k<H, false>(m, vK, sK, t * 16384u); };
    auto VH = [&](auto hh, unsigned p, unsigned n) { if (NOKV == 1) return; if (NOKV == 2) p = 0; constexpr int H = decltype(hh)::value; bool nt; const unsigned t = tile_of(p, nt); const unsigned m = wb + ((n + 2u) & 7u) * 16384u;
      if (nt) dma_cols<H, true>(m, vV, sV, t * 16384u); else dma_cols<H, false>(m, vV, sV, t * 16384u); };
    unsigned co = rot * 128u;
    auto OA = [&](unsigned i) { if (!NOWO || i < 3) dma_wo<0>(wb + wo_slot(i) * 16384u, vWo, sWo, co + (i & 1u) * (128u * 896u)); };
    auto OB = [&](unsigned i) { if (!NOWO || i < 3) dma_wo<1>(wb + wo_slot(i) * 16384u, vWo, sWo, co + (i & 1u) * (128u * 896u)); if (i & 1u) { co += 128u; co = co == 896u ? 0u : co; } };
    unsigned tdummy = 0;
    auto touch = [&]() { const unsigned ta = l < 56u ? b * 28672u + w * 7168u + l * 128u : 0x7ffffff0u;
      asm volatile("buffer_load_dword %0, %1, %2, 0 offen" : "+v"(tdummy) : "v"(ta), "s"(sWo) : "memory"); };
    constexpr std::integral_constant<int, 0> A{}; constexpr std::integral_constant<int, 1> B{};
    asm volatile("s_waitcnt lgkmcnt(0)" : "+s"(epoch) :: "memory");
    vmwait<8>(); lbar();
    WA(4); vmwait<DBH>(); WB(4);
    unsigned sl = 5;
    #pragma nounroll
    for (int s = 1; s < 5; ++s) {
      vmwait<8>(); WA(sl); vmwait<DBH>(); WB(sl); sl = sl == 5u ? 0u : sl + 1u;
      vmwait<8>(); lbar();
      WA(sl); vmwait<DBH>(); WB(sl); sl = sl == 5u ? 0u : sl + 1u;
    }
    vmwait<8>(); WA(sl); vmwait<DBH>(); WB(sl);
    vmwait<8>(); lbar();
    KH(A, 0, 0); vmwait<DB>(); KH(B, 0, 0);
    vmwait<8>(); KH(A, 1, 1); vmwait<DB>(); KH(B, 1, 1);
    vmwait<8>(); lbar();
    KH(A, 2, 2); if (WOT == 3) touch();
    lbar();
    vmwait<DB>(); KH(B, 2, 2);
    vmwait<8>(); KH(A, 3, 3); vmwait<DB>(); KH(B, 3, 3);
    vmwait<8>(); KH(A, 4, 4); if (WOT == 1) touch(); lbar();
    vmwait<DB>(); KH(B, 4, 4);
    vmwait<8>(); VH(A, 0, 5); if (WOT == 2) touch(); lbar();
    vmwait<DB>(); VH(B, 0, 5);
    vmwait<8>(); KH(A, 5, 6); lbar();
    vmwait<DB>(); KH(B, 5, 6);
    constexpr unsigned P0 = 3;
    unsigned sdummy = 0;
    #pragma nounroll
    for (unsigned p = P0; p < 29u; ++p) {
      vmwait<8>(); VH(A, p - 2u, 2u * p + 1u); vmwait<DB>(); VH(B, p - 2u, 2u * p + 1u);
      vmwait<8>(); KH(A, p + 3u, 2u * p + 2u); lbar();
      vmwait<DB>(); KH(B, p + 3u, 2u * p + 2u);
    }
    asm volatile("s_waitcnt lgkmcnt(0)" : "+s"(sdummy), "+v"(tdummy) :: "memory");
    #pragma nounroll
    for (unsigned p = 29; p < 31u; ++p) {
      vmwait<8>(); VH(A, 2u * p - 31u, 2u * p + 1u); vmwait<DB>(); VH(B, 2u * p - 31u, 2u * p + 1u);
      vmwait<8>(); VH(A, 2u * p - 30u, 2u * p + 2u); lbar();
      vmwait<DB>(); VH(B, 2u * p - 30u, 2u * p + 2u);
    }
    vmwait<8>(); VH(A, 31, 63); lbar(); vmwait<DB>(); VH(B, 31, 63);
    vmwait<8>(); OA(0); lbar(); vmwait<DBH>(); OB(0);
    vmwait<8>(); OA(1); lbar(); vmwait<DBH>(); OB(1);
    vmwait<8>(); OA(2); lbar();
    vmwait<DBH>(); OB(2);
    vmwait<8>(); OA(3); vmwait<DBH>(); OB(3);
    #pragma unroll
    for (unsigned c = 0; c < 5u; ++c) {
      vmwait<8>(); OA(2u * c + 4u); if (!COFF) lbar(); vmwait<DBH>(); OB(2u * c + 4u);
      vmwait<8>(); OA(2u * c + 5u); if (COFF) lbar(); vmwait<DBH>(); OB(2u * c + 5u);
    }
    vmwait<8>(); lbar();
    vmwait<4>(); lbar();
    vmwait<0>(); lbar();
    tag = epoch + 0x9E3779B97F4A7C15ull;
  } else {
  CST(0);
  u64 epoch;
  asm volatile("s_load_dwordx2 %0, %1, %2\n\ts_waitcnt lgkmcnt(0)" : "=s"(epoch) : "s"(ws), "s"(grp * 256u) : "memory");
  tag = epoch + 0x9E3779B97F4A7C15ull;
  {
    const u64 pc = pc0;
    unsigned d = 0, o = ((j & 3u) * 4u + w) * 64u;
    unsigned len; asm volatile("s_mov_b32 %0, .Lgqa_end-.Lgqa_start" : "=s"(len));
    #pragma nounroll
    for (; o < len; o += 1024u) asm volatile("s_load_dword %0, %1, %2" : "+s"(d) : "s"(pc), "s"(o) : "memory");
    asm volatile("s_waitcnt lgkmcnt(0)" : "+s"(d) :: "memory");
  }
  const unsigned q16 = l & 15u, g = l >> 4, q32 = l & 31u, h = l >> 5, tp = l & 3u, tr = (l >> 2) & 3u;
  const unsigned perm = 8u * (4u * (tp & 1u) + 2u * (g & 1u) + (tp >> 1));
  f32x16 zero;
  #pragma unroll
  for (int e = 0; e < 16; ++e) zero[e] = 0.f;

  bf16x8 qf[8];
  {
    f32x16 acc[2][2];
    const unsigned wqb = ldsb + h * 4096u + w * 1024u + tr * 64u + perm;
    const unsigned wq2 = opq(wqb + 32768u);
    const unsigned xsw = (q32 >> 1) & 7u;
    const unsigned xb = ldsb + L_XST + (q32 >> 3) * 7168u + (q32 & 7u) * 128u;
    unsigned xo[4];
    #pragma unroll
    for (int t = 0; t < 4; ++t) xo[t] = xb + (((2u * t + h) ^ xsw) * 16u);
    const unsigned qw = ldsb + L_EXT + q32 * 512u;
    unsigned kb = rot;
    bf16x8 xf[2][4][2], wf[2][4][2];
    #pragma unroll
    for (int s = 0; s < 7; ++s) {
      cbar();
      if (s == 0) CST(1);
      const unsigned a = wqb + ((2 * s) % 6) * 16384u, xk = kb * 1024u;
      #pragma unroll
      for (int u = 0; u < (s < 6 ? 4 : 2); ++u) {
        #pragma unroll
        for (int z = 0; z < 4; ++z) {
          if (u == 0) { if (s > 0) acc[z >> 1][z & 1] = mfma32(wf[(s - 1) & 1][3][z >> 1], xf[(s - 1) & 1][3][z & 1], acc[z >> 1][z & 1]); }
          else acc[z >> 1][z & 1] = mfma32(wf[s & 1][u - 1][z >> 1], xf[s & 1][u - 1][z & 1], (s == 0 && u == 1) ? zero : acc[z >> 1][z & 1]);
          PIN();
          if (u < 2) {
            #pragma unroll
            for (int f = 8 * u + 2 * z; f < 8 * u + 2 * z + 2; ++f) {
              const int tt = f >> 2, k = f & 3;
              if (k == 0 || k == 3) { const unsigned aa = (((2 * s) % 6) >= 2 ? wq2 + (((2 * s) % 6) - 2) * 16384u : a) + tt * 8192u + (k & 1) * 512u; wf[s & 1][tt][k & 1] = trfrag(aa, aa + 256u); }
              else xf[s & 1][tt][k - 1] = ldsfrag(xo[tt] + xk + (k - 1) * (4u * 7168u));
            }
            PIN();
          }
        }
      }
      kb = kb == 6u ? 0u : kb + 1u;
    }
    #pragma unroll
    for (int z = 0; z < 5; ++z) {
      #pragma unroll
      for (int t = 1; t < 4; ++t) {
        if (z < 4) acc[z >> 1][z & 1] = mfma32(wf[0][t][z >> 1], xf[0][t][z & 1], acc[z >> 1][z & 1]);
        PIN();
        if (z > 0 && t >= 2) {
          const int y = z - 1, i = t - 2;
          const f32x16& a = acc[y >> 1][y & 1];
          u32x4 v = {cvtpk(a[8 * i], a[8 * i + 1]), cvtpk(a[8 * i + 2], a[8 * i + 3]), cvtpk(a[8 * i + 4], a[8 * i + 5]), cvtpk(a[8 * i + 6], a[8 * i + 7])};
          *(LAS u32x4*)(size_t)(qw + (y & 1) * 16384u + (((8u * w + 4u * (y >> 1) + 2u * h + i) ^ q16) * 16u)) = v;
          PIN();
        }
      }
      if (z == 0) { cbar(); CST(2); }
    }
    CST(3);
  }
  STAMP_AT(1);
  f32x16 O[2][2];
  #pragma unroll
  for (int i = 0; i < 4; ++i) O[i >> 1][i & 1] = zero;
  unsigned kb[4];
  #pragma unroll
  for (int u = 0; u < 4; ++u) kb[u] = ldsb + q16 * 512u + (((4u * u + g) ^ q16) * 16u);
  const unsigned vb = ldsb + w * 1024u + (4u * h + tr) * 64u + 32u * (g & 1u) + 8u * tp;
  const unsigned pw = ldsb + L_EXT + w * 8192u + l * 16u;
  const unsigned pr = ldsb + L_EXT + (g & 1u) * 8192u + (h * 16u + q16) * 16u;
  const unsigned aw = ldsb + L_EXT + w * 8192u + L_AL + q16 * 4u, ar = ldsb + L_EXT + (g & 1u) * 8192u + L_AL + q16 * 4u;
  const float cs = 0.0625f * 1.4426950408889634f;
  float mref = 0.f, lsum = 0.f;

  struct PVF { bf16x8 pf[2][2], vf[2][2]; float a0, a1; };
  auto pv_read = [&](PVF& f, int i, unsigned va, unsigned pa) {
    const int ks = i >> 2, k = i & 3;
    if (k == 0) f.vf[0][ks] = trfrag(va + ks * 4096u, va + ks * 4096u + 8192u);
    if (k == 1) f.pf[0][ks] = ldsfrag(pa + ks * 512u);
    if (k == 2) f.pf[1][ks] = ldsfrag(pa + 16384u + ks * 512u);
    if (k == 3) f.vf[1][ks] = trfrag(va + ks * 4096u + 512u, va + ks * 4096u + 512u + 8192u);
  };
  auto pv_m = [&](const PVF& f, int i) { const int ks = i >> 2, db = (i >> 1) & 1, qb = i & 1; O[db][qb] = mfma32(f.vf[db][ks], f.pf[qb][ks], O[db][qb]); };
  auto rescale = [&](float a0, float a1) {
    if (__builtin_expect(__any(a0 != 1.f || a1 != 1.f), 0)) {
      #pragma unroll
      for (int db = 0; db < 2; ++db)
        #pragma unroll
        for (int e = 0; e < 16; ++e) { O[db][0][e] *= a0; O[db][1][e] *= a1; }
    }
  };
  bf16x8 kf[16];
  auto k_read = [&](int i, const unsigned (&kk)[4]) { kf[i] = ldsfrag(kk[(i >> 1) & 3] + (i >> 3) * 256u + (i & 1) * 8192u); };
  auto phase = [&](auto mode, unsigned kslot, unsigned vslot, unsigned p) {
    constexpr int MODE = decltype(mode)::value;
    constexpr bool FIRST = MODE == 0, PV = MODE >= 2, LAST = MODE == 3;
    PVF f;
    unsigned kk[4], va = 0, pa = 0;
    if constexpr (PV) {
      const unsigned rr = (p + 1u) & 3u;
      va = opq(vb + vslot); pa = opq(pr + rr * 1024u);
      const unsigned aa = ar + rr * 64u; f.a0 = ldsf(aa); f.a1 = ldsf(aa + 16384u);
    }
    PIN();
    f32x4 s0, s1;
    #pragma unroll
    for (int i = 0; i < 16; ++i) {
      if (i == 0) s0 = mfma16(kf[0], qf[0], f32x4{0.f, 0.f, 0.f, 0.f});
      else if (i == 1) s1 = mfma16(kf[1], qf[0], f32x4{0.f, 0.f, 0.f, 0.f});
      else if (i & 1) s1 = mfma16(kf[i], qf[i >> 1], s1);
      else s0 = mfma16(kf[i], qf[i >> 1], s0);
      if constexpr (PV) {
        if (i < 8) pv_read(f, i, va, pa);
        if (!LAST && i >= 4 && i < 8) kk[i - 4] = opq(kb[i - 4] + kslot);
        if (!LAST && i >= 8) k_read(i - 8, kk);
      } else {
        if (i < 8 && (i & 1) == 0) kk[i >> 1] = opq(kb[i >> 1] + kslot);
        k_read(i, kk);
      }
      PIN();
    }
    constexpr bool KR = PV && !LAST;
    asm volatile("" ::: "memory");
    if constexpr (PV) { rescale(f.a0, f.a1); PIN(); pv_m(f, 0); }
    if constexpr (KR) k_read(8, kk);
    PIN();
    float t[8];
    #pragma unroll
    for (int e = 0; e < 4; ++e) t[e] = __builtin_fmaf(s0[e], cs, -mref);
    PIN();
    if constexpr (PV) pv_m(f, 1);
    if constexpr (KR) k_read(9, kk);
    PIN();
    #pragma unroll
    for (int e = 0; e < 4; ++e) t[4 + e] = __builtin_fmaf(s1[e], cs, -mref);
    float mx = fmaxf(fmaxf(t[0], t[1]), fmaxf(t[2], t[3]));
    PIN();
    if constexpr (PV) pv_m(f, 2);
    if constexpr (KR) k_read(10, kk);
    asm volatile("" ::: "memory");
    PIN();
    mx = fmaxf(fmaxf(fmaxf(mx, t[4]), t[5]), fmaxf(t[6], t[7]));
    float alpha = 1.f;
    if (FIRST || __builtin_expect(__any(mx > THR), 0)) {
      auto r = __builtin_amdgcn_permlane16_swap(__float_as_uint(mx), __float_as_uint(mx), false, false);
      mx = fmaxf(__uint_as_float(r[0]), __uint_as_float(r[1]));
      r = __builtin_amdgcn_permlane32_swap(__float_as_uint(mx), __float_as_uint(mx), false, false);
      mx = fmaxf(__uint_as_float(r[0]), __uint_as_float(r[1]));
      if constexpr (!FIRST) { mx = fmaxf(mx, 0.f); alpha = __builtin_amdgcn_exp2f(-mx); lsum *= alpha; }
      mref += mx;
      #pragma unroll
      for (int e = 0; e < 8; ++e) t[e] -= mx;
    }
    PIN();
    #pragma unroll
    for (int z = 0; z < 3; ++z) {
      if constexpr (PV) pv_m(f, 3 + z);
      if constexpr (KR) { k_read(11 + z, kk); if (z == 2) k_read(14, kk); }
      PIN();
      #pragma unroll
      for (int e = 3 * z; e < 3 * z + 3 && e < 8; ++e) { t[e] = __builtin_amdgcn_exp2f(t[e]); asm volatile("" : "+v"(t[e])); }
      PIN();
    }
    if constexpr (PV && !LAST) { pv_m(f, 6); k_read(15, kk); PIN(); }
    if constexpr (LAST) {
      lsum += ((t[0] + t[1]) + (t[2] + t[3])) + ((t[4] + t[5]) + (t[6] + t[7]));
      auto r = __builtin_amdgcn_permlane16_swap(__float_as_uint(lsum), __float_as_uint(lsum), false, false);
      lsum = __uint_as_float(r[0]) + __uint_as_float(r[1]);
      r = __builtin_amdgcn_permlane32_swap(__float_as_uint(lsum), __float_as_uint(lsum), false, false);
      const float li = __builtin_amdgcn_rcpf(__uint_as_float(r[0]) + __uint_as_float(r[1]));
      PIN(); pv_m(f, 6); PIN();
      alpha *= li;
      #pragma unroll
      for (int e = 0; e < 8; ++e) t[e] *= li;
    }
    u32x4 v = {cvtpk(t[0], t[1]), cvtpk(t[2], t[3]), cvtpk(t[4], t[5]), cvtpk(t[6], t[7])};
    *(LAS u32x4*)(size_t)(pw + (p & 3u) * 1024u) = v;
    *(LAS float*)(size_t)(aw + (p & 3u) * 64u) = alpha;
    PIN();
    if constexpr (PV) { pv_m(f, 7); PIN(); }
    if constexpr (!LAST) lsum += ((t[0] + t[1]) + (t[2] + t[3])) + ((t[4] + t[5]) + (t[6] + t[7]));
    PIN();
  };

  CST(4);
  cbar();
  CST(5);
  {
    unsigned kk[4];
    #pragma unroll
    for (int u = 0; u < 4; ++u) kk[u] = opq(kb[u] + 2u * 16384u);
    #pragma unroll
    for (int u = 0; u < 8; ++u) { qf[u] = ldsfrag(kb[u & 3] + L_EXT + w * 8192u + (u >> 2) * 256u); k_read(2 * u, kk); k_read(2 * u + 1, kk); PIN(); }
  }
  CST(6);
  phase(std::integral_constant<int, 0>{}, 3u * 16384u, 0u, 0u);
  CST(7);
  #pragma nounroll
  for (unsigned p = 1; p < 3u; ++p) { cbar(); phase(std::integral_constant<int, 1>{}, (p + 3u) * 16384u, 0u, p); }
  #pragma nounroll
  for (unsigned p = 3; p < 31u; ++p) {
    if (!NOBAR || p >= 29u) cbar();
    phase(std::integral_constant<int, 2>{}, ((2u * p) & 7u) * 16384u, ((2u * p + 1u) & 7u) * 16384u, p);
  }
  cbar();
  CST(8);
  phase(std::integral_constant<int, 3>{}, 0u, 6u * 16384u, 31u);
  const unsigned ost = ldsb + L_EXT + l * 16u;
  const unsigned wob = ldsb + (w & 1u) * 512u + (4u * h + tr) * 64u + perm;
  const unsigned pst = WS_PART + (grp * 16u + mem) * (7u * 8192u) + w * 2048u + l * 16u;
  const unsigned fl = l == 0 ? WS_FLAG + ((grp * 4u + w) * 16u + mem) * FS : 0x7ffffff0u;
  {
    PVF f0, f1, f;
    auto pre = [&](PVF& d, unsigned rr) {
      d.a0 = ldsf(ar + rr * 64u); d.a1 = ldsf(ar + rr * 64u + 16384u);
      #pragma unroll
      for (int i = 0; i < 8; ++i) if ((i & 3) == 1 || (i & 3) == 2) pv_read(d, i, 0u, pr + rr * 1024u);
      PIN();
    };
    auto vrd = [&](PVF& d, unsigned va) {
      #pragma unroll
      for (int i = 0; i < 4; ++i) { pv_read(d, (i >> 1) * 4 + (i & 1) * 3, va, 0u); PIN(); }
    };
    const unsigned v7 = opq(vb + 7u * 16384u), ow = opq(ost + w * 4096u);
    pre(f0, 1u); pre(f1, 2u);
    asm volatile("" :: "v"(wob), "v"(pst), "v"(fl));
    rescale(f0.a0, f0.a1);
    cbar();
    vrd(f0, v7); pre(f, 3u);
    #pragma unroll
    for (int i = 0; i < 8; ++i) { pv_m(f0, i); PIN(); }
    rescale(f1.a0, f1.a1);
    cbar();
    vrd(f1, vb);
    #pragma unroll
    for (int i = 0; i < 8; ++i) { pv_m(f1, (i >> 1) + 4 * (i & 1)); PIN();
      if (i >= 3 && (i & 1)) { const int z = (i - 3) >> 1; O[z >> 1][z & 1] *= (z & 1) ? f.a1 : f.a0; asm volatile("" : "+v"(O[z >> 1][z & 1])); PIN(); } }
    O[1][1] *= f.a1; asm volatile("" : "+v"(O[1][1]));
    CST(9);
    STAMP_AT(2);
    cbar();
    CST(10);
    STAMP_AT(3);
    #pragma unroll
    for (int i = 0; i < 4; ++i) { pv_read(f, (i & 1) * 4 + (i >> 1) * 3, vb + 16384u, 0u); PIN(); }
    pv_m(f, 0); pv_m(f, 4); PIN();
    CST(11);
    #pragma unroll
    for (int z = 0; z < 4; ++z) {
      const f32x16& a = O[z >> 1][z & 1];
      #pragma unroll
      for (int i = 0; i < 2; ++i) {
        if (z < 3) { pv_m(f, z + 1 + 4 * i); PIN(); }
        u32x4 v = {cvtpk(a[8 * i], a[8 * i + 1]), cvtpk(a[8 * i + 2], a[8 * i + 3]), cvtpk(a[8 * i + 4], a[8 * i + 5]), cvtpk(a[8 * i + 6], a[8 * i + 7])};
        *(LAS u32x4*)(size_t)(ow + (z & 1) * 16384u + (2u * (z >> 1) + i) * 1024u) = v;
        PIN();
      }
    }
  }
  CST(12);
  STAMP_AT(4);
  {
    bf16x8 of[16];
    const u32x2 tg = {(unsigned)tag, (unsigned)(tag >> 32)};
    unsigned ch = rot;
    bf16x8 fr[8];
    f32x16 acc[2];
    u32x4 res[2];
    auto pack = [&](int c, int k) {
      const f32x16& a = acc[c & 1];
      res[k >> 2][k & 3] = cvtpk(a[2 * k], a[2 * k + 1]);
      asm volatile("" : "+v"(res[k >> 2]));
    };
    auto put = [&](int i) { if (NOST) asm volatile("" :: "v"(res[i])); else __builtin_amdgcn_raw_buffer_store_b128(res[i], sWs, pst + i * 1024u, ch * 8192u, 16); if (i) ch = ch == 6u ? 0u : ch + 1u; };
    const unsigned ord = ost + (w >> 1) * 16384u;
    unsigned alast = 0;
    #pragma unroll
    for (int c = 0; c < 8; ++c) {
      unsigned a = 0;
      if (c < 7) {
        if (c <= 2) cbar(); else { PIN(); asm volatile("s_barrier" ::: "memory"); PIN(); }
        if (c == 0) CST(13);
        if (c == 6) CST(14);
        a = opq(wob + wo_slot(2 * c) * 16384u);
      }
      if (c == 7) {
        PIN(); asm volatile("s_barrier" ::: "memory"); PIN();
        #pragma unroll
        for (int s = 8; s < 16; ++s) { fr[s & 7] = trfrag(alast + s * 2048u, alast + s * 2048u + 1024u); PIN(); }
      }
      alast = a;
      #pragma unroll
      for (int s = 0; s < 16; ++s) {
        if (s < 8) { if (c > 0) acc[(c - 1) & 1] = mfma32(fr[s], of[8 + s], acc[(c - 1) & 1]); }
        else if (c < 7) acc[c & 1] = mfma32(fr[s & 7], of[s - 8], s == 8 ? zero : acc[c & 1]);
        PIN();
        if (c == 0) of[s] = ldsfrag(ord + s * 1024u);
        if (c < 6 || (c == 6 && s < 8)) fr[s & 7] = trfrag(a + s * 2048u, a + s * 2048u + 1024u);
        PIN();
        if (c > 0 && (s == 10 || s == 11 || s == 13 || s == 14)) { const int k = s < 12 ? 2 * (s - 10) : 2 * (s - 11); pack(c - 1, k); pack(c - 1, k + 1); PIN(); }
        if (c > 0 && (s == 12 || s == 15)) { put(s == 15); PIN(); }
        if (EARLY && c == 7 && s == 1) { vmwait<2 * (6 - EARLY)>(); __builtin_amdgcn_raw_buffer_store_b64(tg, sWs, l == 0 ? fl + 1024u * FS : 0x7ffffff0u, 0, 16); PIN(); }
      }
    }
    CST(15);
    vmwait<0>();
    CST(16);
    __builtin_amdgcn_raw_buffer_store_b64(tg, sWs, fl, 0, 16);
  }
  asm volatile(".Lgqa_end:");
  STAMP_AT(5);
  }
  if (POLL || w8 < 7u) {
    unsigned rc = w8 + rot; rc = w8 == 7u ? 7u : rc >= 7u ? rc - 7u : rc;
    reduce_chunk(sWs, out, grp, mem, rc, l, b, br, tag, (EARLY && w8 < EARLY) ? 1u : 0u, w8 == PWAVE);
    if (w8 == 0u && mem == 0u) {
      u32x2 t = {(unsigned)tag, (unsigned)(tag >> 32)};
      __builtin_amdgcn_raw_buffer_store_b64(t, sWs, l == 0 ? WS_EPOCH + grp * 256u : 0x7ffffff0u, 0, 16);
    }
  }
  STAMP_AT(6);
}

__global__ void amp_spin(const unsigned long long* st, int k) {
  unsigned long long mn = ~0ull, acc = 0, mx = 0;
  const unsigned b = threadIdx.x;
  unsigned long long s0 = st[b * 16], sk = st[b * 16 + (k & 7)];
  __shared__ unsigned long long a0[256], ak[256];
  a0[b] = s0; ak[b] = sk; __syncthreads();
  for (int i = 0; i < 256; ++i) { mn = a0[i] < mn ? a0[i] : mn; mx = ak[i] > mx ? ak[i] : mx; }
  for (int i = 0; i < 256; ++i) acc += ak[i] - mn;
  unsigned long long d = (k & 8) ? (mx - mn) * 256 : acc;
  if (k >= 16) { d = 0; for (int i = 0; i < 256; ++i) d += (st[i * 16 + 8 + (k & 7)] - st[i * 16 + 8]) / 20; }
  d = d * 50 / 256; if (d > 1000000) d = 1000000;
  unsigned long long t0 = __builtin_amdgcn_s_memrealtime();
  while (__builtin_amdgcn_s_memrealtime() - t0 < d) __builtin_amdgcn_s_sleep(8);
}
extern "C" void kernel_launch(void* const* d_in, const int* in_sizes, int n_in, void* d_out, int out_size, void* d_ws, size_t ws_size, hipStream_t stream) {
  (void)in_sizes; (void)n_in; (void)out_size; (void)ws_size;
  (void)hipFuncSetAttribute((const void*)gqa2_fused, hipFuncAttributeMaxDynamicSharedMemorySize, 163840);
  Args a;
  for (int i = 0; i < 10; ++i) a.in[i] = (const char*)d_in[i];
  a.out = (char*)d_out; a.ws = (char*)d_ws;
  hipLaunchKernelGGL(gqa2_fused, dim3(256), dim3(512), 163840, stream, a);
  hipLaunchKernelGGL(amp_spin, dim3(1), dim3(256), 0, stream, (const unsigned long long*)((char*)d_ws + WS_STAMP), 0);
}
```

```cpp
#include <hip/hip_runtime.h>
#include <type_traits>

typedef __bf16   bf16x8 __attribute__((ext_vector_type(8)));
typedef __bf16   bf16x4 __attribute__((ext_vector_type(4)));
typedef __bf16   bf16x2 __attribute__((ext_vector_type(2)));
typedef float    f32x4  __attribute__((ext_vector_type(4)));
typedef float    f32x16 __attribute__((ext_vector_type(16)));
typedef float    f32x2  __attribute__((ext_vector_type(2)));
typedef unsigned u32x4  __attribute__((ext_vector_type(4)));
typedef unsigned u32x2  __attribute__((ext_vector_type(2)));
typedef unsigned long long u64;
typedef __amdgpu_buffer_rsrc_t srd_t;
#define LAS __attribute__((address_space(3)))
#define DI __device__ __forceinline__
#define PIN() __builtin_amdgcn_sched_barrier(0)

#define STAMP 0
#define SWP 0
#define MIXPER 1
#define MIXN 1
#define LOCK_NT 1
#define CODEPF 1
#define ROTK 1
#define WOPF 0
#define PRETOUCH 0
#define NOWO 0
#define NOST 0
#define LAUX 16
#define COFF 1
#define DEEP 0
#define NOCOMP 0
#define KPAT 0
#define NOBAR 0
#define RUNA 0
#define RUNE 0
#define WOT 0
#define NOKV 0
#define BAL 1
#define RED 0
#define EARLY 0
#define NORED 0
#define POLL 1
#define PGAP 2
#define PMODE 0
#define PWAVE 7
#define HEAD2 1
#define TSL 4
#define OST 0
#define TLBW 0
#define DBH 8
#define DB 8

constexpr unsigned WS_EPOCH = 0;
#define FS 8
constexpr unsigned WS_FLAG  = 4096;
constexpr unsigned WS_PART  = 1u << 20;
constexpr unsigned WS_PART_BYTES = 256u * 7u * 8192u;
constexpr unsigned WS_END   = WS_PART + WS_PART_BYTES;
constexpr unsigned WS_STAMP = 32u << 20;
constexpr int NSTAMP = 7, NBAR = 51, LW0 = 0;
constexpr unsigned L_XST = 163840u - 57344u;
constexpr unsigned L_EXT = 131072u;
constexpr unsigned L_AL  = 4096u;
constexpr float THR = 8.0f;
constexpr unsigned wo_slot(unsigned i) { return i < 8u ? (i + 2u) & 7u : i < 10u ? i : i - 8u; }

struct Args { const char* in[10]; char* out; char* ws; };

DI unsigned cvtpk(float lo, float hi) { f32x2 v = {lo, hi}; bf16x2 b = __builtin_convertvector(v, bf16x2); return __builtin_bit_cast(unsigned, b); }
DI f32x4 mfma16(bf16x8 a, bf16x8 b, f32x4 c) { return __builtin_amdgcn_mfma_f32_16x16x32_bf16(a, b, c, 0, 0, 0); }
DI f32x16 mfma32(bf16x8 a, bf16x8 b, f32x16 c) { return __builtin_amdgcn_mfma_f32_32x32x16_bf16(a, b, c, 0, 0, 0); }
DI bf16x8 trfrag(unsigned a0, unsigned a1) {
  bf16x4 lo = __builtin_amdgcn_ds_read_tr16_b64_v4bf16((LAS bf16x4*)(size_t)a0);
  bf16x4 hi = __builtin_amdgcn_ds_read_tr16_b64_v4bf16((LAS bf16x4*)(size_t)a1);
  return __builtin_shufflevector(lo, hi, 0, 1, 2, 3, 4, 5, 6, 7);
}
DI unsigned opq(unsigned a) { asm volatile("" : "+v"(a)); return a; }
DI bf16x8 ldsfrag(unsigned a) { return *(const LAS bf16x8*)(size_t)a; }
DI float ldsf(unsigned a) { return *(const LAS float*)(size_t)a; }
DI void cbar() { PIN(); asm volatile("s_waitcnt lgkmcnt(0)\n\ts_barrier" ::: "memory"); PIN(); }
DI void lbar() { asm volatile("s_barrier" ::: "memory"); }
template <int N> DI void vmwait() { asm volatile("s_waitcnt vmcnt(%0)" :: "i"(N) : "memory"); }

template <int H, bool NT> DI void dma_cols(unsigned m, unsigned v, srd_t srd, unsigned so) {
  if constexpr (NT)
    asm volatile("s_add_u32 m0, %0, %4\n\ts_nop 0\n\tbuffer_load_dwordx4 %1, %2, %3 offen offset:%5 nt lds\n\t"
                 "s_add_u32 m0, %0, %6\n\ts_nop 0\n\tbuffer_load_dwordx4 %1, %2, %3 offen offset:%7 nt lds"
                 :: "s"(m), "v"(v), "s"(srd), "s"(so), "i"(H * 1792), "i"(H * 256), "i"(H * 1792 + 896), "i"(H * 256 + 128) : "memory", "scc");
  else
    asm volatile("s_add_u32 m0, %0, %4\n\ts_nop 0\n\tbuffer_load_dwordx4 %1, %2, %3 offen offset:%5 lds\n\t"
                 "s_add_u32 m0, %0, %6\n\ts_nop 0\n\tbuffer_load_dwordx4 %1, %2, %3 offen offset:%7 lds"
                 :: "s"(m), "v"(v), "s"(srd), "s"(so), "i"(H * 1792), "i"(H * 256), "i"(H * 1792 + 896), "i"(H * 256 + 128) : "memory", "scc");
}
template <int H, bool NT> DI void dma_k(unsigned m, const unsigned (&v)[4], srd_t srd, unsigned so) {
  if constexpr (NT)
    asm volatile("s_mov_b32 m0, %0\n\ts_nop 0\n\tbuffer_load_dwordx4 %1, %3, %4 offen offset:%5 nt lds\n\tbuffer_load_dwordx4 %2, %3, %4 offen offset:%6 nt lds"
                 :: "s"(m), "v"(v[2 * H]), "v"(v[2 * H + 1]), "s"(srd), "s"(so), "i"(H * 2048), "i"(H * 2048 + 1024) : "memory");
  else
    asm volatile("s_mov_b32 m0, %0\n\ts_nop 0\n\tbuffer_load_dwordx4 %1, %3, %4 offen offset:%5 lds\n\tbuffer_load_dwordx4 %2, %3, %4 offen offset:%6 lds"
                 :: "s"(m), "v"(v[2 * H]), "v"(v[2 * H + 1]), "s"(srd), "s"(so), "i"(H * 2048), "i"(H * 2048 + 1024) : "memory");
}
template <int H> DI void dma_wo(unsigned m, unsigned v, srd_t srd, unsigned so) {
  unsigned t;
  asm volatile("s_add_u32 m0, %1, %5\n\ts_add_u32 %0, %4, %6\n\tbuffer_load_dwordx4 %2, %3, %0 offen lds\n\t"
               "s_add_u32 m0, %1, %7\n\ts_add_u32 %0, %4, %8\n\tbuffer_load_dwordx4 %2, %3, %0 offen lds"
               : "=&s"(t) : "s"(m), "v"(v), "s"(srd), "s"(so), "i"(H * 2048), "i"(H * 14336), "i"(H * 2048 + 1024), "i"(H * 14336 + 7168) : "memory", "scc");
}
DI void dma_one(unsigned m, unsigned v, srd_t srd, unsigned so) {
  asm volatile("s_mov_b32 m0, %0\n\ts_nop 0\n\tbuffer_load_dwordx4 %1, %2, %3 offen lds" :: "s"(m), "v"(v), "s"(srd), "s"(so) : "memory");
}

#define STAMP_AT(i) do {} while (0)
#define CST(i) do {} while (0)

#define PSLEEP 1
DI void reduce_chunk(srd_t sWs, char* out, unsigned grp, unsigned mem, unsigned ch, unsigned l, unsigned b, unsigned br, u64 tag, unsigned early, bool poller) {
  const unsigned fa = WS_FLAG + early * (1024u * FS) + ((grp * 4u + (mem >> 2)) * 16u + (l & 15u)) * FS;
  const unsigned pa = WS_PART + (grp * 16u + (l >> 5)) * (7u * 8192u) + ch * 8192u + mem * 512u + (l & 31u) * 16u;
  const unsigned wv = mem >> 2, q = 32u * (wv >> 1) + (l & 31u), n = 64u * ch + 32u * (wv & 1u) + 16u * (mem & 1u) + 8u * ((mem >> 1) & 1u) + 4u * (l >> 5);
  char* const po = out + (size_t)(b * 64u + q) * 1792u + br * 896u + n * 2u;
  asm volatile("" :: "v"(po), "v"(pa));
  u32x2 pend[POLL];
  #pragma unroll
  for (int i = 0; i < POLL; ++i) pend[i] = u32x2{0u, 0u};
  if (poller) {
    #pragma nounroll
    for (unsigned spins = 0; spins < (1u << 20); ++spins) {
      u32x2 fv = {0u, 0u};
      asm volatile("buffer_load_dwordx2 %0, %1, %2, 0 offen sc1\n\ts_waitcnt vmcnt(0)" : "+v"(fv) : "v"(fa), "s"(sWs) : "memory");
      if ((__ballot(fv[0] == (unsigned)tag && fv[1] == (unsigned)(tag >> 32)) & 0xffffu) == 0xffffu) break;
      if (PGAP) __builtin_amdgcn_s_sleep(PGAP);
    }
  }
  asm volatile("s_barrier" ::: "memory");
  if (ch >= 7u) return;
  #pragma nounroll
  for (unsigned spins = 0; spins < ((NORED == 2 || POLL) ? 0u : 1u << 20); ++spins) {
    u32x2 fv;
    asm volatile("buffer_load_dwordx2 %0, %1, %2, 0 offen sc1\n\ts_waitcnt vmcnt(0)" : "=v"(fv) : "v"(fa), "s"(sWs) : "memory");
    if (__all(fv[0] == (unsigned)tag && fv[1] == (unsigned)(tag >> 32))) break;
    if (PSLEEP) __builtin_amdgcn_s_sleep(PSLEEP);
  }
  u32x4 r[8];
  #pragma unroll
  for (int i = 0; i < 8; ++i) r[i] = __builtin_amdgcn_raw_buffer_load_b128(sWs, (NORED == 1 || NORED == 2 || (NORED == 3 && i >= 4) || (NORED == 4 && i >= 1)) ? 0x7ffffff0u : pa, i * (2u * 7u * 8192u), LAUX);
  float a[8];
  #pragma unroll
  for (int i = 0; i < 8; ++i) {
    #pragma unroll
    for (int e = 0; e < 4; ++e) {
      const float lo = __uint_as_float(r[i][e] << 16), hi = __uint_as_float(r[i][e] & 0xffff0000u);
      a[2 * e] = i ? a[2 * e] + lo : lo; a[2 * e + 1] = i ? a[2 * e + 1] + hi : hi;
    }
    PIN();
  }
  #pragma unroll
  for (int i = 0; i < POLL; ++i) asm volatile("" :: "v"(pend[i]));
  float o[4];
  #pragma unroll
  for (int e = 0; e < 4; ++e) {
    auto s = __builtin_amdgcn_permlane32_swap(__float_as_uint(a[e]), __float_as_uint(a[4 + e]), false, false);
    o[e] = __uint_as_float(s[0]) + __uint_as_float(s[1]);
  }
  const u32x2 ov = {cvtpk(o[0], o[1]), cvtpk(o[2], o[3])};
  if (OST == 0) *(u32x2*)po = ov;
  else if (OST == 1) asm volatile("global_store_dwordx2 %0, %1, off sc1" :: "v"(po), "v"(ov) : "memory");
  else if (OST == 2) asm volatile("global_store_dwordx2 %0, %1, off nt" :: "v"(po), "v"(ov) : "memory");
  else asm volatile("global_store_dwordx2 %0, %1, off sc0 sc1" :: "v"(po), "v"(ov) : "memory");
}

__global__ __launch_bounds__(512, 2)
void gqa2_fused(Args args) {
  extern __shared__ __attribute__((aligned(16))) char lds[];
  u64 pc0; asm volatile("s_getpc_b64 %0\n.Lgqa_start:" : "=s"(pc0));
  const unsigned bid = blockIdx.x, br = (bid >> 2) & 1u, j = bid >> 3, hs = j & 1u, b = j >> 2;
  const char* const px = args.in[br * 5u], * const pk = args.in[br * 5u + 1u], * const pv = args.in[br * 5u + 2u], * const pwq = args.in[br * 5u + 3u], * const pwo = args.in[br * 5u + 4u];
  char* const out = args.out; char* const ws = args.ws;
  asm volatile("" :: "s"(px), "s"(pk), "s"(pv), "s"(pwq), "s"(pwo), "s"(out), "s"(ws));
  const unsigned kvh = (bid & 3u) * 2u + ((j >> 1) & 1u), grp = br * 8u + b, mem = kvh * 2u + hs;
  const unsigned tid = threadIdx.x, l = tid & 63u;
  const unsigned w8 = __builtin_amdgcn_readfirstlane(tid >> 6), w = w8 & 3u;
  const unsigned rot = b == 7u ? 3u : b;
  const unsigned ldsb = (unsigned)(size_t)lds;
  STAMP_AT(0);
  const srd_t sWs = __builtin_amdgcn_make_buffer_rsrc((void*)ws, 0, WS_END, 0x00020000);

  u64 tag;
  if (__builtin_expect(w8 < 4u, 1)) {
    const srd_t sWq = __builtin_amdgcn_make_buffer_rsrc((void*)(pwq + mem * 512u), 0, 448 * 8192 - mem * 512u, 0x00020000);
    const unsigned hi = l >> 5, row = (l >> 2) & 7u, col = hi * 64u + (l & 3u) * 16u;
    const unsigned wb = ldsb + w * 4096u;
    const unsigned vWq = (8u * w + row) * 8192u + col;
    unsigned sw = rot << 19;
    auto WA = [&](unsigned slot) { dma_cols<0, false>(wb + slot * 16384u, vWq, sWq, sw); };
    auto WB = [&](unsigned slot) { dma_cols<1, false>(wb + slot * 16384u, vWq, sWq, sw); sw += 0x40000u; sw = sw == 0x380000u ? 0u : sw; };
    WA(0); WB(0); WA(1); WB(1);
    PIN();
    u64 epoch;
    asm volatile("s_load_dwordx2 %0, %1, %2" : "=s"(epoch) : "s"(ws), "s"(grp * 256u) : "memory");
    {
      const srd_t sX = __builtin_amdgcn_make_buffer_rsrc((void*)(px + b * (64u * 896u)), 0, 64 * 896, 0x00020000);
      const unsigned xr = l >> 3;
      const unsigned v0 = xr * 896u + (((l & 7u) ^ (xr >> 1)) * 16u);
      unsigned pc = (j & 3u) * 14u + w;
      #pragma nounroll
      for (int i = 0; i < 14; ++i) {
        const unsigned rg = (pc * 37u) >> 8;
        dma_one(ldsb + L_XST + pc * 1024u, v0 ^ ((rg & 1u) << 6), sX, rg * (7168u - 896u) + pc * 128u);
        pc += 4u; pc = pc >= 56u ? pc - 56u : pc;
      }
    }
    WA(2); WB(2); WA(3); WB(3);
    const size_t kvoff = (size_t)(b * 8u + kvh) * (1024u * 512u);
    const srd_t sK  = __builtin_amdgcn_make_buffer_rsrc((void*)(pk + kvoff), 0, 524288, 0x00020000);
    const srd_t sV  = __builtin_amdgcn_make_buffer_rsrc((void*)(pv + kvoff), 0, 524288, 0x00020000);
    const srd_t sWo = __builtin_amdgcn_make_buffer_rsrc((void*)(pwo + mem * (256u * 896u)), 0, 256 * 896, 0x00020000);
    unsigned vK[4];
    { const unsigned kin = hi * 512u + (((l & 31u) ^ hi) * 16u);
      #pragma unroll
      for (int i = 0; i < 4; ++i) vK[i] = KPAT == 1 ? w * 4096u + l * 16u : w * 4096u + (kin ^ (((4u * w + i) & 7u) * 32u)); }
    const unsigned vV = (8u * w + row) * 512u + col;
    const unsigned vWo = (32u * w + row) * 896u + col;
    const unsigned krot = ROTK == 1 ? (b & 3u) * 8u : ROTK == 2 ? (kvh & 3u) * 8u : 0u;
    auto tile_of = [&](unsigned p, bool& nt) -> unsigned {
      if constexpr (SWP == 0) { nt = LOCK_NT; return (p + krot) & 31u; }
      else {
        const bool sw_ = (((p / (2u * SWP)) + b) % MIXPER) < MIXN;
        nt = sw_ ? ((p & SWP) != 0u) : true;
        return ((sw_ ? (p ^ (hs ? SWP : 0u)) : p) + krot) & 31u;
      }
    };
    auto KH = [&](auto hh, unsigned p, unsigned n) { if (NOKV == 1) return; if (NOKV == 2) p = 0; constexpr int H = decltype(hh)::value; bool nt; const unsigned t = tile_of(p, nt); const unsigned m = wb + ((n + 2u) & 7u) * 16384u;
      if (nt) dma_k<H, true>(m, vK, sK, t * 16384u); else dma_k<H, false>(m, vK, sK, t * 16384u); };
    auto VH = [&](auto hh, unsigned p, unsigned n) { if (NOKV == 1) return; if (NOKV == 2) p = 0; constexpr int H = decltype(hh)::value; bool nt; const unsigned t = tile_of(p, nt); const unsigned m = wb + ((n + 2u) & 7u) * 16384u;
      if (nt) dma_cols<H, true>(m, vV, sV, t * 16384u); else dma_cols<H, false>(m, vV, sV, t * 16384u); };
    unsigned co = rot * 128u;
    auto OA = [&](unsigned i) { if (!NOWO || i < 3) dma_wo<0>(wb + wo_slot(i) * 16384u, vWo, sWo, co + (i & 1u) * (128u * 896u)); };
    auto OB = [&](unsigned i) { if (!NOWO || i < 3) dma_wo<1>(wb + wo_slot(i) * 16384u, vWo, sWo, co + (i & 1u) * (128u * 896u)); if (i & 1u) { co += 128u; co = co == 896u ? 0u : co; } };
    unsigned tdummy = 0;
    auto touch = [&]() { const unsigned ta = l < 56u ? b * 28672u + w * 7168u + l * 128u : 0x7ffffff0u;
      asm volatile("buffer_load_dword %0, %1, %2, 0 offen" : "+v"(tdummy) : "v"(ta), "s"(sWo) : "memory"); };
    constexpr std::integral_constant<int, 0> A{}; constexpr std::integral_constant<int, 1> B{};
    asm volatile("s_waitcnt lgkmcnt(0)" : "+s"(epoch) :: "memory");
    vmwait<8>(); lbar();
    WA(4); vmwait<DBH>(); WB(4);
    unsigned sl = 5;
    #pragma nounroll
    for (int s = 1; s < 5; ++s) {
      vmwait<8>(); WA(sl); vmwait<DBH>(); WB(sl); sl = sl == 5u ? 0u : sl + 1u;
      vmwait<8>(); lbar();
      WA(sl); vmwait<DBH>(); WB(sl); sl = sl == 5u ? 0u : sl + 1u;
    }
    vmwait<8>(); WA(sl); vmwait<DBH>(); WB(sl);
    vmwait<8>(); lbar();
    KH(A, 0, 0); vmwait<DB>(); KH(B, 0, 0);
    vmwait<8>(); KH(A, 1, 1); vmwait<DB>(); KH(B, 1, 1);
    vmwait<8>(); lbar();
    KH(A, 2, 2); if (WOT == 3) touch();
    lbar();
    vmwait<DB>(); KH(B, 2, 2);
    vmwait<8>(); KH(A, 3, 3); vmwait<DB>(); KH(B, 3, 3);
    vmwait<8>(); KH(A, 4, 4); if (WOT == 1) touch(); lbar();
    vmwait<DB>(); KH(B, 4, 4);
    vmwait<8>(); VH(A, 0, 5); if (WOT == 2) touch(); lbar();
    vmwait<DB>(); VH(B, 0, 5);
    vmwait<8>(); KH(A, 5, 6); lbar();
    vmwait<DB>(); KH(B, 5, 6);
    constexpr unsigned P0 = 3;
    unsigned sdummy = 0;
    #pragma nounroll
    for (unsigned p = P0; p < 29u; ++p) {
      vmwait<8>(); VH(A, p - 2u, 2u * p + 1u); vmwait<DB>(); VH(B, p - 2u, 2u * p + 1u);
      vmwait<8>(); KH(A, p + 3u, 2u * p + 2u); lbar();
      vmwait<DB>(); KH(B, p + 3u, 2u * p + 2u);
    }
    asm volatile("s_waitcnt lgkmcnt(0)" : "+s"(sdummy), "+v"(tdummy) :: "memory");
    #pragma nounroll
    for (unsigned p = 29; p < 31u; ++p) {
      vmwait<8>(); VH(A, 2u * p - 31u, 2u * p + 1u); vmwait<DB>(); VH(B, 2u * p - 31u, 2u * p + 1u);
      vmwait<8>(); VH(A, 2u * p - 30u, 2u * p + 2u); lbar();
      vmwait<DB>(); VH(B, 2u * p - 30u, 2u * p + 2u);
    }
    vmwait<8>(); VH(A, 31, 63); lbar(); vmwait<DB>(); VH(B, 31, 63);
    vmwait<8>(); OA(0); lbar(); vmwait<DBH>(); OB(0);
    vmwait<8>(); OA(1); lbar(); vmwait<DBH>(); OB(1);
    vmwait<8>(); if (COFF < 4) OA(2); lbar();
    vmwait<DBH>(); OB(2);
    vmwait<8>(); OA(3); vmwait<DBH>(); OB(3);
    #pragma unroll
    for (unsigned c = 0; c < 5u; ++c) {
      vmwait<8>(); OA(2u * c + 4u); if (!COFF) lbar(); vmwait<DBH>(); OB(2u * c + 4u);
      vmwait<8>(); OA(2u * c + 5u); if (COFF) lbar(); vmwait<DBH>(); OB(2u * c + 5u);
    }
    vmwait<8>(); lbar();
    vmwait<4>(); lbar();
    vmwait<0>(); lbar();
    tag = epoch + 0x9E3779B97F4A7C15ull;
  } else {
  CST(0);
  u64 epoch;
  asm volatile("s_load_dwordx2 %0, %1, %2\n\ts_waitcnt lgkmcnt(0)" : "=s"(epoch) : "s"(ws), "s"(grp * 256u) : "memory");
  tag = epoch + 0x9E3779B97F4A7C15ull;
  {
    const u64 pc = pc0;
    unsigned d = 0, o = ((j & 3u) * 4u + w) * 64u;
    unsigned len; asm volatile("s_mov_b32 %0, .Lgqa_end-.Lgqa_start" : "=s"(len));
    #pragma nounroll
    for (; o < len; o += 1024u) asm volatile("s_load_dword %0, %1, %2" : "+s"(d) : "s"(pc), "s"(o) : "memory");
    asm volatile("s_waitcnt lgkmcnt(0)" : "+s"(d) :: "memory");
  }
  const unsigned q16 = l & 15u, g = l >> 4, q32 = l & 31u, h = l >> 5, tp = l & 3u, tr = (l >> 2) & 3u;
  const unsigned perm = 8u * (4u * (tp & 1u) + 2u * (g & 1u) + (tp >> 1));
  f32x16 zero;
  #pragma unroll
  for (int e = 0; e < 16; ++e) zero[e] = 0.f;

  bf16x8 qf[8];
  {
    f32x16 acc[2][2];
    const unsigned wqb = ldsb + h * 4096u + w * 1024u + tr * 64u + perm;
    const unsigned wq2 = opq(wqb + 32768u);
    const unsigned xsw = (q32 >> 1) & 7u;
    const unsigned xb = ldsb + L_XST + (q32 >> 3) * 7168u + (q32 & 7u) * 128u;
    unsigned xo[4];
    #pragma unroll
    for (int t = 0; t < 4; ++t) xo[t] = xb + (((2u * t + h) ^ xsw) * 16u);
    const unsigned qw = ldsb + L_EXT + q32 * 512u;
    unsigned kb = rot;
    bf16x8 xf[2][4][2], wf[2][4][2];
    #pragma unroll
    for (int s = 0; s < 7; ++s) {
      cbar();
      if (s == 0) CST(1);
      const unsigned a = wqb + ((2 * s) % 6) * 16384u, xk = kb * 1024u;
      #pragma unroll
      for (int u = 0; u < (s < 6 ? 4 : 2); ++u) {
        #pragma unroll
        for (int z = 0; z < 4; ++z) {
          if (u == 0) { if (s > 0) acc[z >> 1][z & 1] = mfma32(wf[(s - 1) & 1][3][z >> 1], xf[(s - 1) & 1][3][z & 1], acc[z >> 1][z & 1]); }
          else acc[z >> 1][z & 1] = mfma32(wf[s & 1][u - 1][z >> 1], xf[s & 1][u - 1][z & 1], (s == 0 && u == 1) ? zero : acc[z >> 1][z & 1]);
          PIN();
          if (u < 2) {
            #pragma unroll
            for (int f = 8 * u + 2 * z; f < 8 * u + 2 * z + 2; ++f) {
              const int tt = f >> 2, k = f & 3;
              if (k == 0 || k == 3) { const unsigned aa = (((2 * s) % 6) >= 2 ? wq2 + (((2 * s) % 6) - 2) * 16384u : a) + tt * 8192u + (k & 1) * 512u; wf[s & 1][tt][k & 1] = trfrag(aa, aa + 256u); }
              else xf[s & 1][tt][k - 1] = ldsfrag(xo[tt] + xk + (k - 1) * (4u * 7168u));
            }
            PIN();
          }
        }
      }
      kb = kb == 6u ? 0u : kb + 1u;
    }
    #pragma unroll
    for (int z = 0; z < 5; ++z) {
      #pragma unroll
      for (int t = 1; t < 4; ++t) {
        if (z < 4) acc[z >> 1][z & 1] = mfma32(wf[0][t][z >> 1], xf[0][t][z & 1], acc[z >> 1][z & 1]);
        PIN();
        if (z > 0 && t >= 2) {
          const int y = z - 1, i = t - 2;
          const f32x16& a = acc[y >> 1][y & 1];
          u32x4 v = {cvtpk(a[8 * i], a[8 * i + 1]), cvtpk(a[8 * i + 2], a[8 * i + 3]), cvtpk(a[8 * i + 4], a[8 * i + 5]), cvtpk(a[8 * i + 6], a[8 * i + 7])};
          *(LAS u32x4*)(size_t)(qw + (y & 1) * 16384u + (((8u * w + 4u * (y >> 1) + 2u * h + i) ^ q16) * 16u)) = v;
          PIN();
        }
      }
      if (z == 0) { cbar(); CST(2); }
    }
    CST(3);
  }
  STAMP_AT(1);
  f32x16 O[2][2];
  #pragma unroll
  for (int i = 0; i < 4; ++i) O[i >> 1][i & 1] = zero;
  unsigned kb[4];
  #pragma unroll
  for (int u = 0; u < 4; ++u) kb[u] = ldsb + q16 * 512u + (((4u * u + g) ^ q16) * 16u);
  const unsigned vb = ldsb + w * 1024u + (4u * h + tr) * 64u + 32u * (g & 1u) + 8u * tp;
  const unsigned pw = ldsb + L_EXT + w * 8192u + l * 16u;
  const unsigned pr = ldsb + L_EXT + (g & 1u) * 8192u + (h * 16u + q16) * 16u;
  const unsigned aw = ldsb + L_EXT + w * 8192u + L_AL + q16 * 4u, ar = ldsb + L_EXT + (g & 1u) * 8192u + L_AL + q16 * 4u;
  const float cs = 0.0625f * 1.4426950408889634f;
  float mref = 0.f, lsum = 0.f;

  struct PVF { bf16x8 pf[2][2], vf[2][2]; float a0, a1; };
  auto pv_read = [&](PVF& f, int i, unsigned va, unsigned pa) {
    const int ks = i >> 2, k = i & 3;
    if (k == 0) f.vf[0][ks] = trfrag(va + ks * 4096u, va + ks * 4096u + 8192u);
    if (k == 1) f.pf[0][ks] = ldsfrag(pa + ks * 512u);
    if (k == 2) f.pf[1][ks] = ldsfrag(pa + 16384u + ks * 512u);
    if (k == 3) f.vf[1][ks] = trfrag(va + ks * 4096u + 512u, va + ks * 4096u + 512u + 8192u);
  };
  auto pv_m = [&](const PVF& f, int i) { const int ks = i >> 2, db = (i >> 1) & 1, qb = i & 1; O[db][qb] = mfma32(f.vf[db][ks], f.pf[qb][ks], O[db][qb]); };
  auto rescale = [&](float a0, float a1) {
    if (__builtin_expect(__any(a0 != 1.f || a1 != 1.f), 0)) {
      #pragma unroll
      for (int db = 0; db < 2; ++db)
        #pragma unroll
        for (int e = 0; e < 16; ++e) { O[db][0][e] *= a0; O[db][1][e] *= a1; }
    }
  };
  bf16x8 kf[16];
  auto k_read = [&](int i, const unsigned (&kk)[4]) { kf[i] = ldsfrag(kk[(i >> 1) & 3] + (i >> 3) * 256u + (i & 1) * 8192u); };
  auto phase = [&](auto mode, unsigned kslot, unsigned vslot, unsigned p) {
    constexpr int MODE = decltype(mode)::value;
    constexpr bool FIRST = MODE == 0, PV = MODE >= 2, LAST = MODE == 3;
    PVF f;
    unsigned kk[4], va = 0, pa = 0;
    if constexpr (PV) {
      const unsigned rr = (p + 1u) & 3u;
      va = opq(vb + vslot); pa = opq(pr + rr * 1024u);
      const unsigned aa = ar + rr * 64u; f.a0 = ldsf(aa); f.a1 = ldsf(aa + 16384u);
    }
    PIN();
    f32x4 s0, s1;
    #pragma unroll
    for (int i = 0; i < 16; ++i) {
      if (i == 0) s0 = mfma16(kf[0], qf[0], f32x4{0.f, 0.f, 0.f, 0.f});
      else if (i == 1) s1 = mfma16(kf[1], qf[0], f32x4{0.f, 0.f, 0.f, 0.f});
      else if (i & 1) s1 = mfma16(kf[i], qf[i >> 1], s1);
      else s0 = mfma16(kf[i], qf[i >> 1], s0);
      if constexpr (PV) {
        if (i < 8) pv_read(f, i, va, pa);
        if (!LAST && i >= 4 && i < 8) kk[i - 4] = opq(kb[i - 4] + kslot);
        if (!LAST && i >= 8) k_read(i - 8, kk);
      } else {
        if (i < 8 && (i & 1) == 0) kk[i >> 1] = opq(kb[i >> 1] + kslot);
        k_read(i, kk);
      }
      PIN();
    }
    constexpr bool KR = PV && !LAST;
    asm volatile("" ::: "memory");
    if constexpr (PV) { rescale(f.a0, f.a1); PIN(); pv_m(f, 0); }
    if constexpr (KR) k_read(8, kk);
    PIN();
    float t[8];
    #pragma unroll
    for (int e = 0; e < 4; ++e) t[e] = __builtin_fmaf(s0[e], cs, -mref);
    PIN();
    if constexpr (PV) pv_m(f, 1);
    if constexpr (KR) k_read(9, kk);
    PIN();
    #pragma unroll
    for (int e = 0; e < 4; ++e) t[4 + e] = __builtin_fmaf(s1[e], cs, -mref);
    float mx = fmaxf(fmaxf(t[0], t[1]), fmaxf(t[2], t[3]));
    PIN();
    if constexpr (PV) pv_m(f, 2);
    if constexpr (KR) k_read(10, kk);
    asm volatile("" ::: "memory");
    PIN();
    mx = fmaxf(fmaxf(fmaxf(mx, t[4]), t[5]), fmaxf(t[6], t[7]));
    float alpha = 1.f;
    if (FIRST || __builtin_expect(__any(mx > THR), 0)) {
      auto r = __builtin_amdgcn_permlane16_swap(__float_as_uint(mx), __float_as_uint(mx), false, false);
      mx = fmaxf(__uint_as_float(r[0]), __uint_as_float(r[1]));
      r = __builtin_amdgcn_permlane32_swap(__float_as_uint(mx), __float_as_uint(mx), false, false);
      mx = fmaxf(__uint_as_float(r[0]), __uint_as_float(r[1]));
      if constexpr (!FIRST) { mx = fmaxf(mx, 0.f); alpha = __builtin_amdgcn_exp2f(-mx); lsum *= alpha; }
      mref += mx;
      #pragma unroll
      for (int e = 0; e < 8; ++e) t[e] -= mx;
    }
    PIN();
    #pragma unroll
    for (int z = 0; z < 3; ++z) {
      if constexpr (PV) pv_m(f, 3 + z);
      if constexpr (KR) { k_read(11 + z, kk); if (z == 2) k_read(14, kk); }
      PIN();
      #pragma unroll
      for (int e = 3 * z; e < 3 * z + 3 && e < 8; ++e) { t[e] = __builtin_amdgcn_exp2f(t[e]); asm volatile("" : "+v"(t[e])); }
      PIN();
    }
    if constexpr (PV && !LAST) { pv_m(f, 6); k_read(15, kk); PIN(); }
    if constexpr (LAST) {
      lsum += ((t[0] + t[1]) + (t[2] + t[3])) + ((t[4] + t[5]) + (t[6] + t[7]));
      auto r = __builtin_amdgcn_permlane16_swap(__float_as_uint(lsum), __float_as_uint(lsum), false, false);
      lsum = __uint_as_float(r[0]) + __uint_as_float(r[1]);
      r = __builtin_amdgcn_permlane32_swap(__float_as_uint(lsum), __float_as_uint(lsum), false, false);
      const float li = __builtin_amdgcn_rcpf(__uint_as_float(r[0]) + __uint_as_float(r[1]));
      PIN(); pv_m(f, 6); PIN();
      alpha *= li;
      #pragma unroll
      for (int e = 0; e < 8; ++e) t[e] *= li;
    }
    u32x4 v = {cvtpk(t[0], t[1]), cvtpk(t[2], t[3]), cvtpk(t[4], t[5]), cvtpk(t[6], t[7])};
    *(LAS u32x4*)(size_t)(pw + (p & 3u) * 1024u) = v;
    *(LAS float*)(size_t)(aw + (p & 3u) * 64u) = alpha;
    PIN();
    if constexpr (PV) { pv_m(f, 7); PIN(); }
    if constexpr (!LAST) lsum += ((t[0] + t[1]) + (t[2] + t[3])) + ((t[4] + t[5]) + (t[6] + t[7]));
    PIN();
  };

  CST(4);
  cbar();
  CST(5);
  {
    unsigned kk[4];
    #pragma unroll
    for (int u = 0; u < 4; ++u) kk[u] = opq(kb[u] + 2u * 16384u);
    #pragma unroll
    for (int u = 0; u < 8; ++u) { qf[u] = ldsfrag(kb[u & 3] + L_EXT + w * 8192u + (u >> 2) * 256u); k_read(2 * u, kk); k_read(2 * u + 1, kk); PIN(); }
  }
  CST(6);
  phase(std::integral_constant<int, 0>{}, 3u * 16384u, 0u, 0u);
  CST(7);
  #pragma nounroll
  for (unsigned p = 1; p < 3u; ++p) { cbar(); phase(std::integral_constant<int, 1>{}, (p + 3u) * 16384u, 0u, p); }
  #pragma nounroll
  for (unsigned p = 3; p < 31u; ++p) {
    if (!NOBAR || p >= 29u) cbar();
    phase(std::integral_constant<int, 2>{}, ((2u * p) & 7u) * 16384u, ((2u * p + 1u) & 7u) * 16384u, p);
  }
  cbar();
  CST(8);
  phase(std::integral_constant<int, 3>{}, 0u, 6u * 16384u, 31u);
  const unsigned ost = ldsb + L_EXT + l * 16u;
  const unsigned wob = ldsb + (w & 1u) * 512u + (4u * h + tr) * 64u + perm;
  const unsigned pst = WS_PART + (grp * 16u + mem) * (7u * 8192u) + w * 2048u + l * 16u;
  const unsigned fl = l == 0 ? WS_FLAG + ((grp * 4u + w) * 16u + mem) * FS : 0x7ffffff0u;
  {
    PVF f0, f1, f;
    auto pre = [&](PVF& d, unsigned rr) {
      d.a0 = ldsf(ar + rr * 64u); d.a1 = ldsf(ar + rr * 64u + 16384u);
      #pragma unroll
      for (int i = 0; i < 8; ++i) if ((i & 3) == 1 || (i & 3) == 2) pv_read(d, i, 0u, pr + rr * 1024u);
      PIN();
    };
    auto vrd = [&](PVF& d, unsigned va) {
      #pragma unroll
      for (int i = 0; i < 4; ++i) { pv_read(d, (i >> 1) * 4 + (i & 1) * 3, va, 0u); PIN(); }
    };
    const unsigned v7 = opq(vb + 7u * 16384u), ow = opq(ost + w * 4096u);
    pre(f0, 1u); pre(f1, 2u);
    asm volatile("" :: "v"(wob), "v"(pst), "v"(fl));
    rescale(f0.a0, f0.a1);
    cbar();
    vrd(f0, v7); pre(f, 3u);
    #pragma unroll
    for (int i = 0; i < 8; ++i) { pv_m(f0, i); PIN(); }
    rescale(f1.a0, f1.a1);
    cbar();
    vrd(f1, vb);
    #pragma unroll
    for (int i = 0; i < 8; ++i) { pv_m(f1, (i >> 1) + 4 * (i & 1)); PIN();
      if (i >= 3 && (i & 1)) { const int z = (i - 3) >> 1; O[z >> 1][z & 1] *= (z & 1) ? f.a1 : f.a0; asm volatile("" : "+v"(O[z >> 1][z & 1])); PIN(); } }
    O[1][1] *= f.a1; asm volatile("" : "+v"(O[1][1]));
    CST(9);
    STAMP_AT(2);
    cbar();
    CST(10);
    STAMP_AT(3);
    #pragma unroll
    for (int i = 0; i < 4; ++i) { pv_read(f, (i & 1) * 4 + (i >> 1) * 3, vb + 16384u, 0u); PIN(); }
    pv_m(f, 0); pv_m(f, 4); PIN();
    CST(11);
    #pragma unroll
    for (int z = 0; z < 4; ++z) {
      const f32x16& a = O[z >> 1][z & 1];
      #pragma unroll
      for (int i = 0; i < 2; ++i) {
        if (z < 3) { pv_m(f, z + 1 + 4 * i); PIN(); }
        u32x4 v = {cvtpk(a[8 * i], a[8 * i + 1]), cvtpk(a[8 * i + 2], a[8 * i + 3]), cvtpk(a[8 * i + 4], a[8 * i + 5]), cvtpk(a[8 * i + 6], a[8 * i + 7])};
        *(LAS u32x4*)(size_t)(ow + (z & 1) * 16384u + (2u * (z >> 1) + i) * 1024u) = v;
        PIN();
      }
    }
  }
  CST(12);
  STAMP_AT(4);
  {
    bf16x8 of[16];
    const u32x2 tg = {(unsigned)tag, (unsigned)(tag >> 32)};
    unsigned ch = rot;
    bf16x8 fr[8];
    f32x16 acc[2];
    u32x4 res[2];
    auto pack = [&](int c, int k) {
      const f32x16& a = acc[c & 1];
      res[k >> 2][k & 3] = cvtpk(a[2 * k], a[2 * k + 1]);
      asm volatile("" : "+v"(res[k >> 2]));
    };
    auto put = [&](int i) { if (NOST) asm volatile("" :: "v"(res[i])); else __builtin_amdgcn_raw_buffer_store_b128(res[i], sWs, pst + i * 1024u, ch * 8192u, 16); if (i) ch = ch == 6u ? 0u : ch + 1u; };
    const unsigned ord = ost + (w >> 1) * 16384u;
    unsigned alast = 0;
    #pragma unroll
    for (int c = 0; c < 8; ++c) {
      unsigned a = 0;
      if (c < 7) {
        if (c <= 2) cbar(); else { PIN(); asm volatile("s_barrier" ::: "memory"); PIN(); }
        if (c == 0) CST(13);
        if (c == 6) CST(14);
        a = opq(wob + wo_slot(2 * c) * 16384u);
      }
      if (c == 7) {
        PIN(); asm volatile("s_barrier" ::: "memory"); PIN();
        #pragma unroll
        for (int s = 8; s < 16; ++s) { fr[s & 7] = trfrag(alast + s * 2048u, alast + s * 2048u + 1024u); PIN(); }
      }
      alast = a;
      #pragma unroll
      for (int s = 0; s < 16; ++s) {
        if (s < 8) { if (c > 0) acc[(c - 1) & 1] = mfma32(fr[s], of[8 + s], acc[(c - 1) & 1]); }
        else if (c < 7) acc[c & 1] = mfma32(fr[s & 7], of[s - 8], s == 8 ? zero : acc[c & 1]);
        PIN();
        if (c == 0) of[s] = ldsfrag(ord + s * 1024u);
        if (c < 6 || (c == 6 && s < 8)) fr[s & 7] = trfrag(a + s * 2048u, a + s * 2048u + 1024u);
        PIN();
        if (c > 0 && (s == 10 || s == 11 || s == 13 || s == 14)) { const int k = s < 12 ? 2 * (s - 10) : 2 * (s - 11); pack(c - 1, k); pack(c - 1, k + 1); PIN(); }
        if (c > 0 && (s == 12 || s == 15)) { put(s == 15); PIN(); }
        if (EARLY && c == 7 && s == 1) { vmwait<2 * (6 - EARLY)>(); __builtin_amdgcn_raw_buffer_store_b64(tg, sWs, l == 0 ? fl + 1024u * FS : 0x7ffffff0u, 0, 16); PIN(); }
      }
    }
    CST(15);
    vmwait<0>();
    CST(16);
    __builtin_amdgcn_raw_buffer_store_b64(tg, sWs, fl, 0, 16);
  }
  asm volatile(".Lgqa_end:");
  STAMP_AT(5);
  }
  if (POLL || w8 < 7u) {
    unsigned rc = w8 + rot; rc = w8 == 7u ? 7u : rc >= 7u ? rc - 7u : rc;
    reduce_chunk(sWs, out, grp, mem, rc, l, b, br, tag, (EARLY && w8 < EARLY) ? 1u : 0u, w8 == PWAVE);
    if (w8 == 0u && mem == 0u) {
      u32x2 t = {(unsigned)tag, (unsigned)(tag >> 32)};
      __builtin_amdgcn_raw_buffer_store_b64(t, sWs, l == 0 ? WS_EPOCH + grp * 256u : 0x7ffffff0u, 0, 16);
    }
  }
  STAMP_AT(6);
}

extern "C" void kernel_launch(void* const* d_in, const int* in_sizes, int n_in, void* d_out, int out_size, void* d_ws, size_t ws_size, hipStream_t stream) {
  (void)in_sizes; (void)n_in; (void)out_size; (void)ws_size;
  (void)hipFuncSetAttribute((const void*)gqa2_fused, hipFuncAttributeMaxDynamicSharedMemorySize, 163840);
  Args a;
  for (int i = 0; i < 10; ++i) a.in[i] = (const char*)d_in[i];
  a.out = (char*)d_out; a.ws = (char*)d_ws;
  hipLaunchKernelGGL(gqa2_fused, dim3(256), dim3(512), 163840, stream, a);
}
```

```cpp
#include <hip/hip_runtime.h>
#include <type_traits>
#include <utility>

typedef __bf16   bf16x8 __attribute__((ext_vector_type(8)));
typedef __bf16   bf16x4 __attribute__((ext_vector_type(4)));
typedef __bf16   bf16x2 __attribute__((ext_vector_type(2)));
typedef float    f32x4  __attribute__((ext_vector_type(4)));
typedef float    f32x16 __attribute__((ext_vector_type(16)));
typedef float    f32x2  __attribute__((ext_vector_type(2)));
typedef unsigned u32x4  __attribute__((ext_vector_type(4)));
typedef unsigned u32x2  __attribute__((ext_vector_type(2)));
typedef unsigned long long u64;
typedef __amdgpu_buffer_rsrc_t srd_t;
#define LAS __attribute__((address_space(3)))
#define DI __device__ __forceinline__
#define PIN() __builtin_amdgcn_sched_barrier(0)
template <int N> using ic = std::integral_constant<int, N>;
template <class F, int... Is> DI void sfor_(F&& f, std::integer_sequence<int, Is...>) { (f(ic<Is>{}), ...); }
template <int N, class F> DI void sfor(F&& f) { sfor_(f, std::make_integer_sequence<int, N>{}); }


constexpr unsigned WS_EPOCH = 0;
constexpr unsigned WS_FLAG  = 4096;
constexpr unsigned WS_PART  = 1u << 20;
constexpr unsigned WS_END   = WS_PART + 256u * 7u * 8192u;
constexpr unsigned OOB = 0x7ffffff0u;
constexpr unsigned L_XST = 163840u - 57344u;
constexpr unsigned L_EXT = 131072u;
constexpr unsigned L_AL  = 4096u;
constexpr float THR = 8.0f;
constexpr unsigned wo_slot(unsigned i) { return i < 12u ? (i + 2u) & 7u : i - 4u; }

struct Args { const char* in[10]; char* out; char* ws; };

DI unsigned cvtpk(float lo, float hi) { f32x2 v = {lo, hi}; bf16x2 b = __builtin_convertvector(v, bf16x2); return __builtin_bit_cast(unsigned, b); }
DI u32x4 pack8(const f32x16& a, int i) { return u32x4{cvtpk(a[8 * i], a[8 * i + 1]), cvtpk(a[8 * i + 2], a[8 * i + 3]), cvtpk(a[8 * i + 4], a[8 * i + 5]), cvtpk(a[8 * i + 6], a[8 * i + 7])}; }
DI f32x4 mfma16(bf16x8 a, bf16x8 b, f32x4 c) { return __builtin_amdgcn_mfma_f32_16x16x32_bf16(a, b, c, 0, 0, 0); }
DI f32x16 mfma32(bf16x8 a, bf16x8 b, f32x16 c) { return __builtin_amdgcn_mfma_f32_32x32x16_bf16(a, b, c, 0, 0, 0); }
DI bf16x8 trfrag(unsigned a0, unsigned a1) {
  bf16x4 lo = __builtin_amdgcn_ds_read_tr16_b64_v4bf16((LAS bf16x4*)(size_t)a0);
  bf16x4 hi = __builtin_amdgcn_ds_read_tr16_b64_v4bf16((LAS bf16x4*)(size_t)a1);
  return __builtin_shufflevector(lo, hi, 0, 1, 2, 3, 4, 5, 6, 7);
}
DI unsigned opq(unsigned a) { asm volatile("" : "+v"(a)); return a; }
DI bf16x8 ldsfrag(unsigned a) { return *(const LAS bf16x8*)(size_t)a; }
DI float ldsf(unsigned a) { return *(const LAS float*)(size_t)a; }
DI void cbar() { PIN(); asm volatile("s_waitcnt lgkmcnt(0)\n\ts_barrier" ::: "memory"); PIN(); }
DI void rbar() { PIN(); asm volatile("s_barrier" ::: "memory"); PIN(); }
DI void lbar() { asm volatile("s_barrier" ::: "memory"); }
template <int N> DI void vmwait() { asm volatile("s_waitcnt vmcnt(%0)" :: "i"(N) : "memory"); }

template <int H> DI void dma_v(unsigned m, unsigned v, srd_t srd, unsigned so) {
  asm volatile("s_mov_b32 m0, %0\n\ts_nop 0\n\tbuffer_load_dwordx4 %1, %2, %3 offen offset:%4 nt lds\n\t"
               "s_add_u32 m0, %0, 768\n\ts_nop 0\n\tbuffer_load_dwordx4 %1, %2, %3 offen offset:%5 nt lds"
               :: "s"(m), "v"(v), "s"(srd), "s"(so), "i"(H * 2048), "i"(H * 2048 + 256) : "memory", "scc");
}
DI void dma_w(unsigned m, unsigned v, srd_t srd, unsigned so) {
  asm volatile("s_mov_b32 m0, %0\n\ts_nop 0\n\tbuffer_load_dwordx4 %1, %2, %3 offen lds\n\t"
               "s_add_u32 m0, %0, 768\n\ts_nop 0\n\tbuffer_load_dwordx4 %1, %2, %3 offen offset:256 lds"
               :: "s"(m), "v"(v), "s"(srd), "s"(so) : "memory", "scc");
}
template <int H> DI void dma_k(unsigned m, const unsigned (&v)[4], srd_t srd, unsigned so) {
  asm volatile("s_mov_b32 m0, %0\n\ts_nop 0\n\tbuffer_load_dwordx4 %1, %3, %4 offen offset:%5 nt lds\n\tbuffer_load_dwordx4 %2, %3, %4 offen offset:%6 nt lds"
               :: "s"(m), "v"(v[2 * H]), "v"(v[2 * H + 1]), "s"(srd), "s"(so), "i"(H * 2048), "i"(H * 2048 + 1024) : "memory");
}
template <int H> DI void dma_wo(unsigned m, unsigned v, srd_t srd, unsigned so) {
  unsigned t;
  asm volatile("s_add_u32 m0, %1, %5\n\ts_add_u32 %0, %4, %6\n\tbuffer_load_dwordx4 %2, %3, %0 offen lds\n\t"
               "s_add_u32 m0, %1, %7\n\ts_add_u32 %0, %4, %8\n\tbuffer_load_dwordx4 %2, %3, %0 offen lds"
               : "=&s"(t) : "s"(m), "v"(v), "s"(srd), "s"(so), "i"(H * 2048), "i"(H * 14336), "i"(H * 2048 + 1024), "i"(H * 14336 + 7168) : "memory", "scc");
}
DI void dma_one(unsigned m, unsigned v, srd_t srd, unsigned so) {
  asm volatile("s_mov_b32 m0, %0\n\ts_nop 0\n\tbuffer_load_dwordx4 %1, %2, %3 offen lds" :: "s"(m), "v"(v), "s"(srd), "s"(so) : "memory");
}
DI unsigned mod7(unsigned x) { return x >= 7u ? x - 7u : x; }


__global__ __launch_bounds__(512, 2)
void gqa2_fused(Args args) {
  extern __shared__ __attribute__((aligned(16))) char lds[];
  u64 pc0; asm volatile("s_getpc_b64 %0\n.Lgqa_start:" : "=s"(pc0));
  const unsigned bid = blockIdx.x, br = (bid >> 2) & 1u, j = bid >> 3, hs = j & 1u, b = j >> 2;
  const char* const px = args.in[br * 5u], * const pk = args.in[br * 5u + 1u], * const pv = args.in[br * 5u + 2u], * const pwq = args.in[br * 5u + 3u], * const pwo = args.in[br * 5u + 4u];
  char* const out = args.out; char* const ws = args.ws;
  asm volatile("" :: "s"(px), "s"(pk), "s"(pv), "s"(pwq), "s"(pwo), "s"(out), "s"(ws));
  const unsigned kvh = (bid & 3u) * 2u + ((j >> 1) & 1u), grp = br * 8u + b, mem = kvh * 2u + hs;
  const unsigned tid = threadIdx.x, l = tid & 63u;
  const unsigned w8 = __builtin_amdgcn_readfirstlane(tid >> 6), w = w8 & 3u;
  const unsigned rot = b == 7u ? 3u : b;
  const unsigned ldsb = (unsigned)(size_t)lds;
  const srd_t sWs = __builtin_amdgcn_make_buffer_rsrc((void*)ws, 0, WS_END, 0x00020000);

  if (__builtin_expect(w8 < 4u, 1)) {
    const srd_t sWq = __builtin_amdgcn_make_buffer_rsrc((void*)(pwq + mem * 512u), 0, 448 * 8192 - mem * 512u, 0x00020000);
    const unsigned hi = l >> 5, row = (l >> 2) & 7u, col = hi * 64u + (l & 3u) * 16u;
    const unsigned wb = ldsb + w * 4096u;
    const unsigned vWq = (8u * w + (row & 3u)) * 8192u + (l >> 4) * 64u + (l & 3u) * 16u;
    unsigned sw = rot << 19;
    auto WA = [&](unsigned slot) { dma_w(wb + slot * 16384u, vWq, sWq, sw); };
    auto WB = [&](unsigned slot) { dma_w(wb + slot * 16384u + 2048u, vWq, sWq, sw + 32768u); sw += 0x40000u; sw = sw == 0x380000u ? 0u : sw; };
    WA(0); WB(0); WA(1); WB(1);
    PIN();
    {
      const srd_t sX = __builtin_amdgcn_make_buffer_rsrc((void*)(px + b * (64u * 896u)), 0, 64 * 896, 0x00020000);
      const unsigned xr = l >> 3;
      const unsigned v0 = xr * 896u + (((l & 7u) ^ (xr >> 1)) * 16u);
      unsigned pc = (j & 3u) * 14u + w;
      #pragma nounroll
      for (int i = 0; i < 14; ++i) {
        const unsigned rg = (pc * 37u) >> 8;
        dma_one(ldsb + L_XST + pc * 1024u, v0 ^ ((rg & 1u) << 6), sX, rg * (7168u - 896u) + pc * 128u);
        pc += 4u; pc = pc >= 56u ? pc - 56u : pc;
      }
    }
    WA(2); WB(2); WA(3); WB(3);
    const size_t kvoff = (size_t)(b * 8u + kvh) * (1024u * 512u);
    const srd_t sK  = __builtin_amdgcn_make_buffer_rsrc((void*)(pk + kvoff), 0, 524288, 0x00020000);
    const srd_t sV  = __builtin_amdgcn_make_buffer_rsrc((void*)(pv + kvoff), 0, 524288, 0x00020000);
    const srd_t sWo = __builtin_amdgcn_make_buffer_rsrc((void*)(pwo + mem * (256u * 896u)), 0, 256 * 896, 0x00020000);
    unsigned vK[4];
    { const unsigned kin = hi * 512u + (((l & 31u) ^ hi) * 16u);
      #pragma unroll
      for (int i = 0; i < 4; ++i) vK[i] = w * 32768u + (kin ^ (((4u * w + i) & 7u) * 32u)); }
    const unsigned vV = w * 32768u + (row & 3u) * 512u + (l >> 4) * 64u + (l & 3u) * 16u;
    const unsigned vWo = (32u * w + row) * 896u + col;
    const unsigned kxor = ((b & 1u) << 13) | ((b & 2u) << 17) | ((b & 4u) << 10) | (br << 14);
    auto koff = [&](unsigned p) {
      return (((p & 1u) << 17) | ((p & 2u) << 12) | ((p & 4u) << 10) | ((p & 8u) << 11) | ((p & 16u) << 14)) ^ kxor;
    };
    auto KH = [&](auto hh, unsigned p, unsigned n) { dma_k<decltype(hh)::value>(wb + ((n + 2u) & 7u) * 16384u, vK, sK, koff(p)); };
    auto VH = [&](auto hh, unsigned p, unsigned n) { dma_v<decltype(hh)::value>(wb + ((n + 2u) & 7u) * 16384u, vV, sV, koff(p)); };
    auto OH = [&](auto hh, unsigned i) {
      const unsigned c = i >= 12u ? 6u : (i >> 2) * 2u + (i & 1u), half = i >= 12u ? i & 1u : (i >> 1) & 1u;
      dma_wo<decltype(hh)::value>(wb + wo_slot(i) * 16384u, vWo, sWo, mod7(rot + c) * 128u + half * (128u * 896u));
    };
    constexpr ic<0> A{}; constexpr ic<1> B{};
    vmwait<8>(); lbar();
    WA(4); vmwait<8>(); WB(4);
    unsigned sl = 5;
    #pragma nounroll
    for (int s = 1; s < 5; ++s) {
      vmwait<8>(); WA(sl); vmwait<8>(); WB(sl); sl = sl == 5u ? 0u : sl + 1u;
      vmwait<8>(); lbar();
      WA(sl); vmwait<8>(); WB(sl); sl = sl == 5u ? 0u : sl + 1u;
    }
    vmwait<8>(); WA(sl); vmwait<8>(); WB(sl);
    vmwait<8>(); lbar();
    KH(A, 0, 0); vmwait<8>(); KH(B, 0, 0);
    vmwait<8>(); KH(A, 1, 1); vmwait<8>(); KH(B, 1, 1);
    vmwait<8>(); lbar();
    KH(A, 2, 2);
    lbar();
    vmwait<8>(); KH(B, 2, 2);
    vmwait<8>(); KH(A, 3, 3); vmwait<8>(); KH(B, 3, 3);
    vmwait<8>(); KH(A, 4, 4); lbar();
    vmwait<8>(); KH(B, 4, 4);
    vmwait<8>(); VH(A, 0, 5); lbar();
    vmwait<8>(); VH(B, 0, 5);
    vmwait<8>(); KH(A, 5, 6); lbar();
    vmwait<8>(); KH(B, 5, 6);
    #pragma nounroll
    for (unsigned p = 3; p < 29u; ++p) {
      vmwait<8>(); VH(A, p - 2u, 2u * p + 1u); vmwait<8>(); VH(B, p - 2u, 2u * p + 1u);
      vmwait<8>(); KH(A, p + 3u, 2u * p + 2u); lbar();
      vmwait<8>(); KH(B, p + 3u, 2u * p + 2u);
    }
    #pragma nounroll
    for (unsigned p = 29; p < 31u; ++p) {
      vmwait<8>(); VH(A, 2u * p - 31u, 2u * p + 1u); vmwait<8>(); VH(B, 2u * p - 31u, 2u * p + 1u);
      vmwait<8>(); VH(A, 2u * p - 30u, 2u * p + 2u); lbar();
      vmwait<8>(); VH(B, 2u * p - 30u, 2u * p + 2u);
    }
    #define TILE(i) vmwait<8>(); OH(A, i); vmwait<8>(); OH(B, i);
    vmwait<8>(); VH(A, 31, 63); vmwait<8>(); VH(B, 31, 63); lbar();
    TILE(0) vmwait<8>(); OH(A, 1); lbar();
    vmwait<8>(); OH(B, 1); vmwait<8>(); OH(A, 2); lbar();
    vmwait<8>(); OH(B, 2); lbar();
    TILE(3) TILE(4) TILE(5) TILE(6) lbar();
    TILE(7) lbar();
    TILE(8) TILE(9) lbar();
    TILE(10) TILE(11) lbar();
    TILE(12) TILE(13) lbar();
    vmwait<8>(); lbar();
    vmwait<4>(); lbar();
    vmwait<0>(); lbar();
    #undef TILE
  } else {
    u64 tag;
    asm volatile("s_load_dwordx2 %0, %1, %2\n\ts_waitcnt lgkmcnt(0)" : "=s"(tag) : "s"(ws), "s"(WS_EPOCH + grp * 256u) : "memory");
    tag += 0x9E3779B97F4A7C15ull;
    {
      unsigned d = 0, o = ((j & 3u) * 4u + w) * 64u;
      unsigned len; asm volatile("s_mov_b32 %0, .Lgqa_end-.Lgqa_start" : "=s"(len));
      #pragma nounroll
      for (; o < len; o += 1024u) asm volatile("s_load_dword %0, %1, %2" : "+s"(d) : "s"(pc0), "s"(o) : "memory");
      asm volatile("s_waitcnt lgkmcnt(0)" : "+s"(d) :: "memory");
    }
    const unsigned q16 = l & 15u, g = l >> 4, q32 = l & 31u, h = l >> 5, tp = l & 3u, tr = (l >> 2) & 3u;
    const unsigned perm = 8u * (4u * (tp & 1u) + 2u * (g & 1u) + (tp >> 1));
    f32x16 zero;
    #pragma unroll
    for (int e = 0; e < 16; ++e) zero[e] = 0.f;

    {
      f32x16 acc[2][2];
      const unsigned wqb = ldsb + h * 4096u + w * 512u + tr * 64u + perm;
      const unsigned wq2 = opq(wqb + 32768u);
      const unsigned xsw = (q32 >> 1) & 7u;
      const unsigned xb = ldsb + L_XST + (q32 >> 3) * 7168u + (q32 & 7u) * 128u;
      unsigned xo[4];
      #pragma unroll
      for (int t = 0; t < 4; ++t) xo[t] = xb + (((2u * t + h) ^ xsw) * 16u);
      const unsigned qw = ldsb + L_EXT + q32 * 512u;
      unsigned kb = rot;
      bf16x8 xf[2][4][2], wf[2][4][2];
      #pragma unroll
      for (int s = 0; s < 7; ++s) {
        cbar();
        const unsigned a = wqb + ((2 * s) % 6) * 16384u, xk = kb * 1024u;
        #pragma unroll
        for (int u = 0; u < (s < 6 ? 4 : 2); ++u) {
          #pragma unroll
          for (int z = 0; z < 4; ++z) {
            if (u == 0) { if (s > 0) acc[z >> 1][z & 1] = mfma32(wf[(s - 1) & 1][3][z >> 1], xf[(s - 1) & 1][3][z & 1], acc[z >> 1][z & 1]); }
            else acc[z >> 1][z & 1] = mfma32(wf[s & 1][u - 1][z >> 1], xf[s & 1][u - 1][z & 1], (s == 0 && u == 1) ? zero : acc[z >> 1][z & 1]);
            PIN();
            if (u < 2) {
              #pragma unroll
              for (int f = 8 * u + 2 * z; f < 8 * u + 2 * z + 2; ++f) {
                const int tt = f >> 2, k = f & 3;
                if (k == 0 || k == 3) { const unsigned aa = (((2 * s) % 6) >= 2 ? wq2 + (((2 * s) % 6) - 2) * 16384u : a) + tt * 8192u + (k & 1) * 256u; wf[s & 1][tt][k & 1] = trfrag(aa, aa + 2048u); }
                else xf[s & 1][tt][k - 1] = ldsfrag(xo[tt] + xk + (k - 1) * (4u * 7168u));
              }
              PIN();
            }
          }
        }
        kb = kb == 6u ? 0u : kb + 1u;
      }
      #pragma unroll
      for (int z = 0; z < 5; ++z) {
        #pragma unroll
        for (int t = 1; t < 4; ++t) {
          if (z < 4) acc[z >> 1][z & 1] = mfma32(wf[0][t][z >> 1], xf[0][t][z & 1], acc[z >> 1][z & 1]);
          PIN();
          if (z > 0 && t >= 2) {
            const int y = z - 1, i = t - 2;
            *(LAS u32x4*)(size_t)(qw + (y & 1) * 16384u + (((8u * w + 4u * (y >> 1) + 2u * h + i) ^ q16) * 16u)) = pack8(acc[y >> 1][y & 1], i);
            PIN();
          }
        }
        if (z == 0) { cbar(); }
      }
    }
    f32x16 O[2][2];
    #pragma unroll
    for (int i = 0; i < 4; ++i) O[i >> 1][i & 1] = zero;
    unsigned kb[4];
    #pragma unroll
    for (int u = 0; u < 4; ++u) kb[u] = ldsb + q16 * 512u + (((4u * u + g) ^ q16) * 16u);
    const unsigned vb = ldsb + h * 2048u + w * 512u + tr * 64u + 32u * (g & 1u) + 8u * tp;
    const unsigned pw = ldsb + L_EXT + w * 8192u + l * 16u;
    const unsigned pr = ldsb + L_EXT + (g & 1u) * 8192u + (h * 16u + q16) * 16u;
    const unsigned aw = ldsb + L_EXT + w * 8192u + L_AL + q16 * 4u, ar = ldsb + L_EXT + (g & 1u) * 8192u + L_AL + q16 * 4u;
    const float cs = 0.0625f * 1.4426950408889634f;
    float mref = 0.f, lsum = 0.f;
    bf16x8 qf[8], kf[16];

    struct PVF { bf16x8 pf[2][2], vf[2][2]; float a0, a1; };
    auto pv_read = [&](PVF& f, int i, unsigned va, unsigned pa) {
      const int ks = i >> 2, k = i & 3;
      if (k == 0) f.vf[0][ks] = trfrag(va + ks * 4096u, va + ks * 4096u + 8192u);
      if (k == 1) f.pf[0][ks] = ldsfrag(pa + ks * 512u);
      if (k == 2) f.pf[1][ks] = ldsfrag(pa + 16384u + ks * 512u);
      if (k == 3) f.vf[1][ks] = trfrag(va + ks * 4096u + 256u, va + ks * 4096u + 256u + 8192u);
    };
    auto pv_m = [&](const PVF& f, int i) { const int ks = i >> 2, db = (i >> 1) & 1, qb = i & 1; O[db][qb] = mfma32(f.vf[db][ks], f.pf[qb][ks], O[db][qb]); };
    auto rescale = [&](float a0, float a1) {
      if (__builtin_expect(__any(a0 != 1.f || a1 != 1.f), 0)) {
        #pragma unroll
        for (int db = 0; db < 2; ++db) { O[db][0] *= a0; O[db][1] *= a1; }
      }
    };
    auto k_read = [&](int i, const unsigned (&kk)[4]) { kf[i] = ldsfrag(kk[(i >> 1) & 3] + (i >> 3) * 256u + (i & 1) * 8192u); };
    auto phase = [&](auto mode, unsigned kslot, unsigned vslot, unsigned p) {
      constexpr int MODE = decltype(mode)::value;
      constexpr bool FIRST = MODE == 0, PV = MODE >= 2, LAST = MODE == 3, KR = PV && !LAST;
      PVF f;
      unsigned kk[4], va = 0, pa = 0;
      if constexpr (PV) {
        const unsigned rr = (p + 1u) & 3u;
        va = opq(vb + vslot); pa = opq(pr + rr * 1024u);
        const unsigned aa = ar + rr * 64u; f.a0 = ldsf(aa); f.a1 = ldsf(aa + 16384u);
      }
      PIN();
      f32x4 s0, s1;
      #pragma unroll
      for (int i = 0; i < 16; ++i) {
        if (i == 0) s0 = mfma16(kf[0], qf[0], f32x4{0.f, 0.f, 0.f, 0.f});
        else if (i == 1) s1 = mfma16(kf[1], qf[0], f32x4{0.f, 0.f, 0.f, 0.f});
        else if (i & 1) s1 = mfma16(kf[i], qf[i >> 1], s1);
        else s0 = mfma16(kf[i], qf[i >> 1], s0);
        if constexpr (PV) {
          if (i < 8) pv_read(f, i, va, pa);
          if (KR && i >= 4 && i < 8) kk[i - 4] = opq(kb[i - 4] + kslot);
          if (KR && i >= 8) k_read(i - 8, kk);
        } else {
          if (i < 8 && (i & 1) == 0) kk[i >> 1] = opq(kb[i >> 1] + kslot);
          k_read(i, kk);
        }
        PIN();
      }
      asm volatile("" ::: "memory");
      if constexpr (PV) { rescale(f.a0, f.a1); PIN(); pv_m(f, 0); }
      if constexpr (KR) k_read(8, kk);
      PIN();
      float t[8];
      #pragma unroll
      for (int e = 0; e < 4; ++e) t[e] = __builtin_fmaf(s0[e], cs, -mref);
      PIN();
      if constexpr (PV) pv_m(f, 1);
      if constexpr (KR) k_read(9, kk);
      PIN();
      #pragma unroll
      for (int e = 0; e < 4; ++e) t[4 + e] = __builtin_fmaf(s1[e], cs, -mref);
      float mx = fmaxf(fmaxf(t[0], t[1]), fmaxf(t[2], t[3]));
      PIN();
      if constexpr (PV) pv_m(f, 2);
      if constexpr (KR) k_read(10, kk);
      asm volatile("" ::: "memory");
      PIN();
      mx = fmaxf(fmaxf(fmaxf(mx, t[4]), t[5]), fmaxf(t[6], t[7]));
      float alpha = 1.f;
      if (FIRST || __builtin_expect(__any(mx > THR), 0)) {
        auto r = __builtin_amdgcn_permlane16_swap(__float_as_uint(mx), __float_as_uint(mx), false, false);
        mx = fmaxf(__uint_as_float(r[0]), __uint_as_float(r[1]));
        r = __builtin_amdgcn_permlane32_swap(__float_as_uint(mx), __float_as_uint(mx), false, false);
        mx = fmaxf(__uint_as_float(r[0]), __uint_as_float(r[1]));
        if constexpr (!FIRST) { mx = fmaxf(mx, 0.f); alpha = __builtin_amdgcn_exp2f(-mx); lsum *= alpha; }
        mref += mx;
        #pragma unroll
        for (int e = 0; e < 8; ++e) t[e] -= mx;
      }
      PIN();
      #pragma unroll
      for (int z = 0; z < 3; ++z) {
        if constexpr (PV) pv_m(f, 3 + z);
        if constexpr (KR) { k_read(11 + z, kk); if (z == 2) k_read(14, kk); }
        PIN();
        #pragma unroll
        for (int e = 3 * z; e < 3 * z + 3 && e < 8; ++e) { t[e] = __builtin_amdgcn_exp2f(t[e]); asm volatile("" : "+v"(t[e])); }
        PIN();
      }
      if constexpr (KR) { pv_m(f, 6); k_read(15, kk); PIN(); }
      if constexpr (LAST) {
        lsum += ((t[0] + t[1]) + (t[2] + t[3])) + ((t[4] + t[5]) + (t[6] + t[7]));
        auto r = __builtin_amdgcn_permlane16_swap(__float_as_uint(lsum), __float_as_uint(lsum), false, false);
        lsum = __uint_as_float(r[0]) + __uint_as_float(r[1]);
        r = __builtin_amdgcn_permlane32_swap(__float_as_uint(lsum), __float_as_uint(lsum), false, false);
        const float li = __builtin_amdgcn_rcpf(__uint_as_float(r[0]) + __uint_as_float(r[1]));
        PIN(); pv_m(f, 6); PIN();
        alpha *= li;
        #pragma unroll
        for (int e = 0; e < 8; ++e) t[e] *= li;
      }
      *(LAS u32x4*)(size_t)(pw + (p & 3u) * 1024u) = u32x4{cvtpk(t[0], t[1]), cvtpk(t[2], t[3]), cvtpk(t[4], t[5]), cvtpk(t[6], t[7])};
      *(LAS float*)(size_t)(aw + (p & 3u) * 64u) = alpha;
      PIN();
      if constexpr (PV) { pv_m(f, 7); PIN(); }
      if constexpr (!LAST) lsum += ((t[0] + t[1]) + (t[2] + t[3])) + ((t[4] + t[5]) + (t[6] + t[7]));
      PIN();
    };
    cbar();
    {
      unsigned kk[4];
      #pragma unroll
      for (int u = 0; u < 4; ++u) kk[u] = opq(kb[u] + 2u * 16384u);
      #pragma unroll
      for (int u = 0; u < 8; ++u) { qf[u] = ldsfrag(kb[u & 3] + L_EXT + w * 8192u + (u >> 2) * 256u); k_read(2 * u, kk); k_read(2 * u + 1, kk); PIN(); }
    }
    phase(ic<0>{}, 3u * 16384u, 0u, 0u);
    #pragma nounroll
    for (unsigned p = 1; p < 3u; ++p) { cbar(); phase(ic<1>{}, (p + 3u) * 16384u, 0u, p); }
    #pragma nounroll
    for (unsigned p = 3; p < 31u; ++p) { cbar(); phase(ic<2>{}, ((2u * p) & 7u) * 16384u, ((2u * p + 1u) & 7u) * 16384u, p); }
    cbar();
    phase(ic<3>{}, 0u, 6u * 16384u, 31u);
    const unsigned ost = ldsb + L_EXT + l * 16u;
    const unsigned wob = ldsb + (w & 1u) * 512u + (4u * h + tr) * 64u + perm;
    const unsigned pst = WS_PART + (grp * 16u + mem) * (7u * 8192u) + w * 2048u + l * 16u;
    {
      PVF f0, f1, f;
      auto pre = [&](PVF& d, unsigned rr) {
        d.a0 = ldsf(ar + rr * 64u); d.a1 = ldsf(ar + rr * 64u + 16384u);
        #pragma unroll
        for (int i = 0; i < 8; ++i) if ((i & 3) == 1 || (i & 3) == 2) pv_read(d, i, 0u, pr + rr * 1024u);
        PIN();
      };
      auto vrd = [&](PVF& d, unsigned va) {
        #pragma unroll
        for (int i = 0; i < 4; ++i) { pv_read(d, (i >> 1) * 4 + (i & 1) * 3, va, 0u); PIN(); }
      };
      const unsigned v7 = opq(vb + 7u * 16384u), ow = opq(ost + w * 4096u);
      pre(f0, 1u); pre(f1, 2u);
      asm volatile("" :: "v"(wob), "v"(pst));
      rescale(f0.a0, f0.a1);
      cbar();
      vrd(f0, v7); pre(f, 3u);
      #pragma unroll
      for (int i = 0; i < 8; ++i) { pv_m(f0, i); PIN(); }
      rescale(f1.a0, f1.a1);
      cbar();
      vrd(f1, vb);
      #pragma unroll
      for (int i = 0; i < 8; ++i) { pv_m(f1, (i >> 1) + 4 * (i & 1)); PIN();
        if (i >= 3 && (i & 1)) { const int z = (i - 3) >> 1; O[z >> 1][z & 1] *= (z & 1) ? f.a1 : f.a0; asm volatile("" : "+v"(O[z >> 1][z & 1])); PIN(); } }
      O[1][1] *= f.a1; asm volatile("" : "+v"(O[1][1]));
      cbar();
      #pragma unroll
      for (int i = 0; i < 4; ++i) { pv_read(f, (i & 1) * 4 + (i >> 1) * 3, vb + 16384u, 0u); PIN(); }
      pv_m(f, 0); pv_m(f, 4); PIN();
      #pragma unroll
      for (int z = 0; z < 4; ++z) {
        #pragma unroll
        for (int i = 0; i < 2; ++i) {
          if (z < 3) { pv_m(f, z + 1 + 4 * i); PIN(); }
          *(LAS u32x4*)(size_t)(ow + (z & 1) * 16384u + (2u * (z >> 1) + i) * 1024u) = pack8(O[z >> 1][z & 1], i);
          PIN();
        }
      }
    }
    {
      bf16x8 of[2][16], fr[8];
      f32x16 acc[2][2];
      u32x4 res[14];
      const unsigned o0 = ost + (w >> 1) * 16384u, o1 = ost + ((w >> 1) ^ 1u) * 16384u;
      const unsigned wo2 = wob + (w >> 1) * 16384u;
      auto put = [&](const f32x16& a, int u, int k) {
        u32x4& r = res[2 * u + (k >> 1)];
        r[2 * (k & 1)] = cvtpk(a[4 * k], a[4 * k + 1]); r[2 * (k & 1) + 1] = cvtpk(a[4 * k + 2], a[4 * k + 3]);
        asm volatile("" : "+v"(r));
        PIN();
        if (k & 1) { __builtin_amdgcn_raw_buffer_store_b128(r, sWs, pst + (k >> 1) * 1024u, u * 8192u, 16); PIN(); }
      };
      unsigned a = 0;
      sfor<60>([&](auto fc) {
        constexpr int F = decltype(fc)::value, M = F - 4, ks = M & 15, pp = M >> 4;
        if constexpr (F <= 48 && (F & 7) == 0) {
          constexpr int c = F / 8;
          if constexpr (c <= 2) cbar(); else rbar();
          a = opq((c == 6 ? wob : wo2) + wo_slot(2 * c) * 16384u);
        }
        if constexpr (M >= 0) { acc[pp & 1][0] = mfma32(fr[M & 7], of[0][ks], ks == 0 ? zero : acc[pp & 1][0]); PIN(); }
        if constexpr (F < 16) of[0][F] = ldsfrag(o0 + F * 1024u);
        if constexpr (F < 56) fr[F & 7] = trfrag(a + (F & 7) * 2048u, a + (F & 7) * 2048u + 1024u);
        PIN();
        if constexpr (M >= 0 && M < 48) { acc[pp & 1][1] = mfma32(fr[M & 7], of[1][ks], ks == 0 ? zero : acc[pp & 1][1]); PIN(); }
        if constexpr (F < 16) { of[1][F] = ldsfrag(o1 + F * 1024u); PIN(); }
        if constexpr (M >= 16 && M < 48 && ks >= 1 && ks < 9) put(acc[(pp - 1) & 1][(ks - 1) >> 2], 2 * pp - 2 + ((ks - 1) >> 2), (ks - 1) & 3);
        if constexpr (M >= 50 && M < 54) { put(acc[0][(M - 50) >> 1], 4 + ((M - 50) >> 1), 2 * (M & 1)); put(acc[0][(M - 50) >> 1], 4 + ((M - 50) >> 1), 2 * (M & 1) + 1); }
      });
      rbar();
      #pragma unroll
      for (int k = 8; k < 16; ++k) { fr[k & 7] = trfrag(a + k * 2048u, a + k * 2048u + 1024u); PIN(); }
      #pragma unroll
      for (int k = 8; k < 16; ++k) { acc[1][0] = mfma32(fr[k & 7], of[0][k], acc[1][0]); PIN(); }
      #pragma unroll
      for (int k = 0; k < 4; ++k) put(acc[1][0], 6, k);
      vmwait<0>();
      const u32x2 tg = {(unsigned)tag, (unsigned)(tag >> 32)};
      __builtin_amdgcn_raw_buffer_store_b64(tg, sWs, l == 0 ? WS_FLAG + ((grp * 4u + w) * 16u + mem) * 8u : OOB, 0, 16);
      if (w == 3u) {
        const unsigned fa = WS_FLAG + ((grp * 4u + (mem >> 2)) * 16u + q16) * 8u;
        #pragma nounroll
        for (unsigned spins = 0; spins < (1u << 20); ++spins) {
          u32x2 fv;
          asm volatile("buffer_load_dwordx2 %0, %1, %2, 0 offen sc1\n\ts_waitcnt vmcnt(0)" : "=v"(fv) : "v"(fa), "s"(sWs) : "memory");
          if (__all(fv[0] == tg[0] && fv[1] == tg[1])) break;
          __builtin_amdgcn_s_sleep(2);
        }
        rbar();
        if (mem == 0u)
          __builtin_amdgcn_raw_buffer_store_b64(tg, sWs, l == 0 ? WS_EPOCH + grp * 256u : OOB, 0, 16);
        asm volatile(".Lgqa_end:");
        return;
      }
    }
  }
  {
    const unsigned u = w8, wv = mem >> 2;
    const unsigned pa = WS_PART + (grp * 16u + (l >> 5)) * (7u * 8192u) + u * 8192u + mem * 512u + (l & 31u) * 16u;
    const unsigned ch = mod7(rot + (u == 6u ? 6u : (u & ~1u) + (wv >> 1))), qb = (wv >> 1) ^ (u & 1u);
    const unsigned n = 64u * ch + 32u * (wv & 1u) + 16u * (mem & 1u) + 8u * ((mem >> 1) & 1u) + 4u * (l >> 5);
    char* const po = out + (size_t)(b * 64u + 32u * qb + (l & 31u)) * 1792u + br * 896u + n * 2u;
    asm volatile("" :: "v"(po), "v"(pa));
    rbar();
    u32x4 r[8];
    #pragma unroll
    for (int i = 0; i < 8; ++i) r[i] = __builtin_amdgcn_raw_buffer_load_b128(sWs, pa, i * (2u * 7u * 8192u), 18);
    float a[8];
    #pragma unroll
    for (int i = 0; i < 8; ++i) {
      #pragma unroll
      for (int e = 0; e < 4; ++e) {
        const float lo = __uint_as_float(r[i][e] << 16), hi = __uint_as_float(r[i][e] & 0xffff0000u);
        a[2 * e] = i ? a[2 * e] + lo : lo; a[2 * e + 1] = i ? a[2 * e + 1] + hi : hi;
      }
      PIN();
    }
    float o[4];
    #pragma unroll
    for (int e = 0; e < 4; ++e) {
      auto s = __builtin_amdgcn_permlane32_swap(__float_as_uint(a[e]), __float_as_uint(a[4 + e]), false, false);
      o[e] = __uint_as_float(s[0]) + __uint_as_float(s[1]);
    }
    *(u32x2*)po = u32x2{cvtpk(o[0], o[1]), cvtpk(o[2], o[3])};
  }
}

extern "C" void kernel_launch(void* const* d_in, const int* in_sizes, int n_in, void* d_out, int out_size, void* d_ws, size_t ws_size, hipStream_t stream) {
  (void)in_sizes; (void)n_in; (void)out_size; (void)ws_size;
  (void)hipFuncSetAttribute((const void*)gqa2_fused, hipFuncAttributeMaxDynamicSharedMemorySize, 163840);
  Args a;
  for (int i = 0; i < 10; ++i) a.in[i] = (const char*)d_in[i];
  a.out = (char*)d_out; a.ws = (char*)d_ws;
  hipLaunchKernelGGL(gqa2_fused, dim3(256), dim3(512), 163840, stream, a);
}
```
